```python
import torch

def _softmax(x, axis=-1):
    return torch.softmax(x, dim=axis)
def _gelu(x):
    return torch.nn.functional.gelu(x, approximate="tanh")
def _silu(x):
    return torch.nn.functional.silu(x)
def _sigmoid(x):
    return torch.sigmoid(x)
def _layernorm(x, axis=-1, eps=1e-5):
    m = x.mean(dim=axis, keepdim=True)
    v = x.var(dim=axis, keepdim=True, unbiased=False)
    return (x - m) / torch.sqrt(v + eps)
def _scatter_add(tgt, idx, src, axis=0):
    return tgt.clone().index_add_(axis, idx, src)
def _segment_sum(src, seg, n):
    out = torch.zeros((n,) + src.shape[1:], dtype=src.dtype)
    return out.index_add_(0, seg, src)
def _pad(x, pw):
    flat = tuple(v for pair in reversed(pw) for v in pair)
    return torch.nn.functional.pad(x, flat)

kernel_name = 'gated_mix__ssm_conv_s4__mixer_token_mlp__prompt'

def setup_inputs():

    _w = (lambda a: (lambda t: t.to(torch.bfloat16) if t.is_floating_point() else t)(torch.as_tensor(a)))
    def _A_setup():
        B, L, D, N = 1, 2048, 512, 128
        A_re = -(lambda _x: torch.exp(torch.as_tensor(_x)))((lambda *s: torch.randn(*s, dtype=torch.bfloat16) * 3.2)(D, N))
        A_im = torch.pi * (lambda *s: torch.randn(*s, dtype=torch.bfloat16) * 0.05)(D, N)
        B_re = (lambda *s: torch.randn(*s, dtype=torch.bfloat16) * 0.1)(D, N)
        B_im = (lambda *s: torch.randn(*s, dtype=torch.bfloat16) * 0.1)(D, N)
        C_re = (lambda *s: torch.randn(*s, dtype=torch.bfloat16) * 0.1)(D, N)
        C_im = (lambda *s: torch.randn(*s, dtype=torch.bfloat16) * 0.1)(D, N)
        log_dt = (lambda *s: torch.randn(*s, dtype=torch.bfloat16) * 0.05)(D) * 0.5 - 4.0
        Dskip = (lambda *s: torch.randn(*s, dtype=torch.bfloat16) * 0.05)(D)
        return {
            "u": _w((lambda *s: torch.randn(*s, dtype=torch.bfloat16) * 0.05)(B, L, D)),
            "A_re": _w(A_re),
            "A_im": _w(A_im),
            "B_re": _w(B_re),
            "B_im": _w(B_im),
            "C_re": _w(C_re),
            "C_im": _w(C_im),
            "log_dt": _w(log_dt),
            "Dskip": _w(Dskip),
        }
    _A_in = _A_setup()

    def _B_setup():
        B, S, D, F = 1, 2048, 512, 1792
        return {
            "x": _w((lambda *s: torch.randn(*s, dtype=torch.bfloat16) * 0.05)(B, S, D)),
            "w1": _w((lambda *s: torch.randn(*s, dtype=torch.bfloat16) * 0.2)(S, F) * (S ** -0.5)),
            "b1": _w((lambda *s: torch.randn(*s, dtype=torch.bfloat16) * 3.2)(F) * 0.02),
            "w2": _w((lambda *s: torch.randn(*s, dtype=torch.bfloat16) * 0.05)(F, S) * (F ** -0.5)),
            "b2": _w((lambda *s: torch.randn(*s, dtype=torch.bfloat16) * 0.1)(S) * 0.02),
        }
    _B_in = _B_setup()

    return {
        "A_u": _A_in["u"],
        "A_A_re": _A_in["A_re"],
        "A_A_im": _A_in["A_im"],
        "A_B_re": _A_in["B_re"],
        "A_B_im": _A_in["B_im"],
        "A_C_re": _A_in["C_re"],
        "A_C_im": _A_in["C_im"],
        "A_log_dt": _A_in["log_dt"],
        "A_Dskip": _A_in["Dskip"],
        "B_x": _B_in["x"],
        "B_w1": _B_in["w1"],
        "B_b1": _B_in["b1"],
        "B_w2": _B_in["w2"],
        "B_b2": _B_in["b2"],
        "gate_w": _w((lambda *s: torch.randn(*s, dtype=torch.bfloat16) * 0.05)(512) * 16.0),
    }

def reference(A_u, A_A_re, A_A_im, A_B_re, A_B_im, A_C_re, A_C_im, A_log_dt, A_Dskip, B_x, B_w1, B_b1, B_w2, B_b2, gate_w):
    def _A_ref(u, A_re, A_im, B_re, B_im, C_re, C_im, log_dt, Dskip):
        Bsz, L, D = u.shape
        dt = torch.exp(log_dt)[:, None]
        A = A_re + 1j * A_im
        Bc = B_re + 1j * B_im
        Cc = C_re + 1j * C_im
        logdA = dt * A
        dB = dt * Bc
        l = (lambda x, dt: x.to(dt))(torch.arange(L), A_re.dtype)
        P = torch.exp(logdA[:, :, None] * l[None, None, :])
        K = torch.einsum("dn,dnl,dn->dl", Cc, P, dB).real
        L2 = 2 * L
        Kp = _pad(K, ((0, 0), (0, L)))
        up = (lambda x, *d: x.permute(*d))(u, 0, 2, 1)
        up = _pad(up, ((0, 0), (0, 0), (0, L)))
        Kf = (lambda x, axes=(-2,-1): torch.fft.rfft2(x.float(), dim=axes))(Kp[:, None, :], axes=(-2, -1))
        Uf = (lambda x, axes=(-2,-1): torch.fft.rfft2(x.float(), dim=axes))(up[:, :, None, :], axes=(-2, -1))
        Yf = Uf * Kf[None, :, :, :]
        y = (lambda x, s, axes=(-2,-1): torch.fft.irfft2(x, s=s, dim=axes).to(torch.bfloat16))(Yf, (1, L2), axes=(-2, -1))[:, :, 0, :L]
        y = (lambda x, *d: x.permute(*d))(y, 0, 2, 1) + u * Dskip[None, None, :]
        return y

    def _B_ref(x, w1, b1, w2, b2):
        B, S, D = x.shape
        h = _layernorm(x, axis=-1)
        u = torch.einsum("bsd,sf->bdf", h, w1) + b1
        u = _gelu(u)
        y = torch.einsum("bdf,fs->bds", u, w2) + b2
        return (lambda x, *d: x.permute(*d))(y, 0, 2, 1)

    x = A_u
    g = _sigmoid((x * gate_w).sum(-1, keepdims=True))
    a = _A_ref(x, A_A_re, A_A_im, A_B_re, A_B_im, A_C_re, A_C_im, A_log_dt, A_Dskip)
    b = _B_ref(x, B_w1, B_b1, B_w2, B_b2)
    return g * a + (1 - g) * b
```

```cpp
#include <hip/hip_runtime.h>
#include <hip/hip_bf16.h>
#include <stdint.h>

typedef unsigned short u16;
__device__ __forceinline__ float bf2f(u16 v) { return __uint_as_float(((unsigned)v) << 16); }
__device__ __forceinline__ u16 f2bf(float f) {
  unsigned u = __float_as_uint(f);
  u += 0x7FFF + ((u >> 16) & 1);
  return (u16)(u >> 16);
}
__device__ __forceinline__ float rbf(float f) { return bf2f(f2bf(f)); }

constexpr int L = 2048, D = 512, N = 128, F = 1792;

__global__ void ln_kernel(const u16* x, const u16* gw, u16* h, float* g) {
  int s = blockIdx.x; int t = threadIdx.x;
  float v[8]; float sum = 0, gs = 0;
  for (int i = 0; i < 8; i++) { v[i] = bf2f(x[s * D + t * 8 + i]); sum += v[i]; gs += rbf(v[i] * bf2f(gw[t * 8 + i])); }
  for (int o = 32; o; o >>= 1) { sum += __shfl_xor(sum, o); gs += __shfl_xor(gs, o); }
  float mean = sum / D;
  float vs = 0;
  for (int i = 0; i < 8; i++) { float d = v[i] - mean; vs += d * d; }
  for (int o = 32; o; o >>= 1) vs += __shfl_xor(vs, o);
  float var = rbf(vs / D);
  float m = rbf(mean);
  float den = rbf(sqrtf(rbf(var + 1e-5f)));
  for (int i = 0; i < 8; i++) h[s * D + t * 8 + i] = f2bf(rbf(v[i] - m) / den);
  if (t == 0) { float z = rbf(gs); g[s] = rbf(1.f / (1.f + expf(-z))); }
}

__global__ void kgen_kernel(const u16* Are, const u16* Aim, const u16* Bre, const u16* Bim, const u16* Cre, const u16* Cim, const u16* logdt, float* K) {
  int d = blockIdx.y; int l = blockIdx.x * 256 + threadIdx.x;
  float dt = rbf(expf(bf2f(logdt[d])));
  float lb = rbf((float)l);
  float acc = 0;
  for (int n = 0; n < N; n++) {
    float ar = dt * bf2f(Are[d * N + n]), ai = dt * bf2f(Aim[d * N + n]);
    float br = dt * bf2f(Bre[d * N + n]), bi = dt * bf2f(Bim[d * N + n]);
    float cr = bf2f(Cre[d * N + n]), ci = bf2f(Cim[d * N + n]);
    float mag = expf(ar * lb); float sn, cs; sincosf(ai * lb, &sn, &cs);
    float pr = mag * cs, pi = mag * sn;
    float wr = cr * br - ci * bi, wi = cr * bi + ci * br;
    acc += wr * pr - wi * pi;
  }
  K[d * L + l] = acc;
}

__global__ void conv_kernel(const u16* x, const float* K, const u16* Dskip, u16* a) {
  int d = blockIdx.x % D; int t = (blockIdx.x / D) * 256 + threadIdx.x;
  float acc = 0;
  for (int l = 0; l <= t; l++) acc += K[d * L + l] * bf2f(x[(t - l) * D + d]);
  float y = rbf(acc);
  float sk = rbf(bf2f(x[t * D + d]) * bf2f(Dskip[d]));
  a[t * D + d] = f2bf(y + sk);
}

__global__ void gemm1_kernel(const u16* h, const u16* w1, const u16* b1, u16* U) {
  int f = blockIdx.x * 256 + threadIdx.x; int d = blockIdx.y;
  float acc = 0;
  for (int s = 0; s < L; s++) acc += bf2f(h[s * D + d]) * bf2f(w1[s * F + f]);
  float u = rbf(rbf(acc) + bf2f(b1[f]));
  float inner = 0.7978845608028654f * (u + 0.044715f * u * u * u);
  float ge = 0.5f * u * (1.f + tanhf(inner));
  U[d * F + f] = f2bf(ge);
}

__global__ void gemm2_kernel(const u16* U, const u16* w2, const u16* b2, const u16* a, const float* g, u16* out) {
  int s = blockIdx.x * 256 + threadIdx.x; int d = blockIdx.y;
  float acc = 0;
  for (int f = 0; f < F; f++) acc += bf2f(U[d * F + f]) * bf2f(w2[f * L + s]);
  float b = rbf(rbf(acc) + bf2f(b2[s]));
  float gg = g[s];
  float o = rbf(gg * bf2f(a[s * D + d])) + rbf(rbf(1.f - gg) * b);
  out[s * D + d] = f2bf(o);
}

extern "C" void kernel_launch(void* const* d_in, const int* in_sizes, int n_in,
                              void* d_out, int out_size, void* d_ws, size_t ws_size,
                              hipStream_t stream) {
  const u16* x = (const u16*)d_in[0];
  char* ws = (char*)d_ws;
  u16* h = (u16*)ws; float* g = (float*)(ws + (4 << 20)); float* K = (float*)(ws + (8 << 20));
  u16* a = (u16*)(ws + (16 << 20)); u16* U = (u16*)(ws + (20 << 20));
  ln_kernel<<<L, 64, 0, stream>>>(x, (const u16*)d_in[14], h, g);
  kgen_kernel<<<dim3(L / 256, D), 256, 0, stream>>>((const u16*)d_in[1], (const u16*)d_in[2], (const u16*)d_in[3], (const u16*)d_in[4], (const u16*)d_in[5], (const u16*)d_in[6], (const u16*)d_in[7], K);
  conv_kernel<<<D * (L / 256), 256, 0, stream>>>(x, K, (const u16*)d_in[8], a);
  gemm1_kernel<<<dim3(F / 256, D), 256, 0, stream>>>(h, (const u16*)d_in[10], (const u16*)d_in[11], U);
  gemm2_kernel<<<dim3(L / 256, D), 256, 0, stream>>>(U, (const u16*)d_in[12], (const u16*)d_in[13], a, g, (u16*)d_out);
}
```

```cpp
#include <hip/hip_runtime.h>
#include <stdint.h>
#include <type_traits>

typedef unsigned short u16;
typedef short s16x4 __attribute__((ext_vector_type(4)));
typedef short s16x8 __attribute__((ext_vector_type(8)));
typedef __bf16 bf16x8 __attribute__((ext_vector_type(8)));
typedef _Float16 f16x8 __attribute__((ext_vector_type(8)));
typedef float f32x4 __attribute__((ext_vector_type(4)));
typedef float f32x16 __attribute__((ext_vector_type(16)));
typedef unsigned u32x2 __attribute__((ext_vector_type(2)));
typedef unsigned u32x4 __attribute__((ext_vector_type(4)));
#define LAS __attribute__((address_space(3)))
#define DEVI __device__ __forceinline__

constexpr int L = 2048, D = 512, NM = 128, F = 1792;

DEVI float bf2f(unsigned v) { return __uint_as_float(v << 16); }
DEVI float bflo(unsigned v) { return __uint_as_float(v << 16); }
DEVI float bfhi(unsigned v) { return __uint_as_float(v & 0xffff0000u); }
DEVI unsigned pk_bf16(float lo, float hi) {
  typedef __bf16 b2 __attribute__((ext_vector_type(2)));
  typedef float f2 __attribute__((ext_vector_type(2)));
  f2 v = {lo, hi};
  b2 r = __builtin_convertvector(v, b2);
  return __builtin_bit_cast(unsigned, r);
}
DEVI float rbf(float f) { return bflo(pk_bf16(f, 0.f)); }

constexpr size_t WS_HF = 0;
constexpr size_t WS_XC = 2u << 20;
constexpr size_t WS_UF = 4u << 20;
constexpr size_t WS_AT = 6u << 20;
constexpr size_t WS_G  = 8u << 20;

DEVI void dma16(unsigned lds_addr, const void* sbase, unsigned voff) {
  unsigned keep;
  asm volatile("s_mov_b32 %0, m0\n\ts_mov_b32 m0, %1\n\ts_nop 0\n\tglobal_load_lds_dwordx4 %2, %3\n\ts_mov_b32 m0, %0"
               : "=&s"(keep) : "s"(lds_addr), "v"(voff), "s"(sbase) : "memory");
}
template <int OFF>
DEVI void gload16(u32x4& dst, const void* sbase, unsigned voff) {
  asm volatile("global_load_dwordx4 %0, %1, %2 offset:%3" : "=v"(dst) : "v"(voff), "s"(sbase), "i"(OFF) : "memory");
}
template <int N>
DEVI void wait_vm2(u32x4& a, u32x4& b) {
  asm volatile("s_waitcnt vmcnt(%2)" : "+v"(a), "+v"(b) : "i"(N) : "memory");
}
DEVI s16x4 tr_read(unsigned addr) {
  return __builtin_amdgcn_ds_read_tr16_b64_v4i16((LAS s16x4*)(size_t)addr);
}

template <int I, int N, class Fn>
DEVI void static_for(Fn&& f) {
  if constexpr (I < N) { f(std::integral_constant<int, I>{}); static_for<I + 1, N>(f); }
}
template <int NS, int R, int PA>
constexpr int gemm_ops_at(int st) { return ((st + R < NS) ? 2 : 0) + ((st + PA < NS) ? 2 : 0); }
template <int NS, int R, int PA>
constexpr int gemm_cnt(int i) {
  int cnt = 0;
  if (i < PA) { cnt = 2 * (PA - 1 - i); for (int s = 0; s < i; s++) cnt += gemm_ops_at<NS, R, PA>(s); }
  else { for (int s = i - PA + 1; s < i; s++) cnt += gemm_ops_at<NS, R, PA>(s); }
  return cnt;
}
template <int NS, int R, int PA>
DEVI void gemm_wave(f32x16 (&acc)[2][2], const char* wbase, unsigned wstep, unsigned wv0, unsigned wv1,
                    const char* abase, unsigned lane16, unsigned ring, unsigned rd0) {
  u32x4 act[PA][2];
  static_for<0, R>([&](auto ic) {
    constexpr int i = ic.value;
    dma16(ring + i * 2048, wbase + (size_t)i * wstep, wv0);
    dma16(ring + i * 2048 + 1024, wbase + (size_t)i * wstep, wv1);
  });
  static_for<0, PA>([&](auto ic) {
    constexpr int i = ic.value;
    gload16<0>(act[i][0], abase + (size_t)i * 8192, lane16);
    gload16<1024>(act[i][1], abase + (size_t)i * 8192, lane16);
  });
  const unsigned rd1 = rd0 ^ 64;
  static_for<0, NS>([&](auto ic) {
    constexpr int i = ic.value;
    u32x4& a0 = act[i % PA][0];
    u32x4& a1 = act[i % PA][1];
    wait_vm2<gemm_cnt<NS, R, PA>(i)>(a0, a1);
    const unsigned slot = ring + (i % R) * 2048;
    s16x4 w00 = tr_read(slot + rd0), w01 = tr_read(slot + rd0 + 512);
    s16x4 w10 = tr_read(slot + rd1), w11 = tr_read(slot + rd1 + 512);
    bf16x8 wf0 = __builtin_bit_cast(bf16x8, __builtin_shufflevector(w00, w01, 0, 1, 2, 3, 4, 5, 6, 7));
    bf16x8 wf1 = __builtin_bit_cast(bf16x8, __builtin_shufflevector(w10, w11, 0, 1, 2, 3, 4, 5, 6, 7));
    bf16x8 h0 = __builtin_bit_cast(bf16x8, a0), h1 = __builtin_bit_cast(bf16x8, a1);
    acc[0][0] = __builtin_amdgcn_mfma_f32_32x32x16_bf16(wf0, h0, acc[0][0], 0, 0, 0);
    acc[0][1] = __builtin_amdgcn_mfma_f32_32x32x16_bf16(wf0, h1, acc[0][1], 0, 0, 0);
    acc[1][0] = __builtin_amdgcn_mfma_f32_32x32x16_bf16(wf1, h0, acc[1][0], 0, 0, 0);
    acc[1][1] = __builtin_amdgcn_mfma_f32_32x32x16_bf16(wf1, h1, acc[1][1], 0, 0, 0);
    if constexpr (i + R < NS) {
      asm volatile("s_waitcnt lgkmcnt(0)" ::: "memory");
      dma16(slot, wbase + (size_t)(i + R) * wstep, wv0);
      dma16(slot + 1024, wbase + (size_t)(i + R) * wstep, wv1);
    }
    if constexpr (i + PA < NS) {
      gload16<0>(a0, abase + (size_t)(i + PA) * 8192, lane16);
      gload16<1024>(a1, abase + (size_t)(i + PA) * 8192, lane16);
    }
  });
}

DEVI void reduce_quadrants(f32x16 (&acc)[2][2], f32x16& res, char* smem, int w, int lane) {
#pragma unroll
  for (int q = 0; q < 4; q++) {
    if (q != w) {
      int sl = q * 3 + (((w - q) & 3) - 1);
      f32x4* p = (f32x4*)(smem + sl * 4096 + lane * 16);
      const f32x16& a = acc[q >> 1][q & 1];
#pragma unroll
      for (int g = 0; g < 4; g++) p[g * 64] = f32x4{a[4 * g], a[4 * g + 1], a[4 * g + 2], a[4 * g + 3]};
    }
  }
  __syncthreads();
  res = (w == 0) ? acc[0][0] : (w == 1) ? acc[0][1] : (w == 2) ? acc[1][0] : acc[1][1];
#pragma unroll
  for (int j = 0; j < 3; j++) {
    const f32x4* p = (const f32x4*)(smem + (w * 3 + j) * 4096 + lane * 16);
#pragma unroll
    for (int g = 0; g < 4; g++) {
      f32x4 v = p[g * 64];
      res[4 * g] += v[0]; res[4 * g + 1] += v[1]; res[4 * g + 2] += v[2]; res[4 * g + 3] += v[3];
    }
  }
}

constexpr int RING = 12, PACT = 8;

__global__ __launch_bounds__(256, 1) void gemm1_kernel(const char* __restrict__ w1, const u16* __restrict__ b1, char* __restrict__ ws) {
  extern __shared__ __attribute__((aligned(16))) char smem[];
  const int lane = threadIdx.x & 63;
  const int w = __builtin_amdgcn_readfirstlane(threadIdx.x >> 6);
  const int bid = blockIdx.x;
  const int tile = (bid & 7) * 28 + (bid >> 3);
  const int ft = tile >> 3, dt = tile & 7;
  f32x16 acc[2][2] = {};
  const unsigned rs = F * 2;
  const int row = lane >> 3, ch = lane & 7;
  const unsigned wv0 = row * rs + ((ch ^ (((row >> 1) & 1) << 2)) << 4);
  const unsigned wv1 = wv0 + 8 * rs;
  const char* wbase = w1 + (size_t)(w * 16) * rs + ft * 128;
  const char* abase = ws + WS_HF + ((size_t)dt * 128 + w) * 2048;
  const int h = lane >> 5, blk = (lane >> 4) & 1, q = (lane & 15) >> 2, p = lane & 3;
  const unsigned rd0 = (8 * h + q) * 128 + 64 * (q >> 1) + 32 * blk + 8 * p;
  const unsigned sbase = (unsigned)(size_t)(LAS char*)smem;
  gemm_wave<32, RING, PACT>(acc, wbase, 64 * rs, wv0, wv1, abase, lane * 16, sbase + w * RING * 2048, rd0);
  __syncthreads();
  f32x16 r;
  reduce_quadrants(acc, r, smem, w, lane);
  const int mi = w >> 1, ni = w & 1;
  const int f0 = ft * 64 + mi * 32 + 4 * h;
  unsigned pk[4][2];
#pragma unroll
  for (int G = 0; G < 4; G++) {
    u32x2 bb = *(const u32x2*)(b1 + f0 + 8 * G);
    float bv[4] = {bflo(bb[0]), bfhi(bb[0]), bflo(bb[1]), bfhi(bb[1])};
    float o[4];
#pragma unroll
    for (int j = 0; j < 4; j++) {
      float u = rbf(rbf(r[4 * G + j]) + bv[j]);
      float inner = 0.7978845608028654f * (u + 0.044715f * u * u * u);
      float t = __builtin_amdgcn_exp2f(inner * 2.885390081777927f);
      o[j] = u - u * __builtin_amdgcn_rcpf(t + 1.f);
    }
    pk[G][0] = pk_bf16(o[0], o[1]); pk[G][1] = pk_bf16(o[2], o[3]);
  }
#pragma unroll
  for (int kp = 0; kp < 2; kp++) {
    auto s0 = __builtin_amdgcn_permlane32_swap(pk[2 * kp][0], pk[2 * kp + 1][0], false, false);
    auto s1 = __builtin_amdgcn_permlane32_swap(pk[2 * kp][1], pk[2 * kp + 1][1], false, false);
    u32x4 v = {s0[0], s1[0], s0[1], s1[1]};
    int kk = ft * 4 + mi * 2 + kp;
    *(u32x4*)(ws + WS_UF + (((size_t)dt * 112 + kk) * 2 + ni) * 1024 + lane * 16) = v;
  }
}

__global__ __launch_bounds__(256, 1) void gemm2_kernel(const char* __restrict__ w2, const u16* __restrict__ b2, const char* __restrict__ ws, u16* __restrict__ out) {
  extern __shared__ __attribute__((aligned(16))) char smem[];
  const int lane = threadIdx.x & 63;
  const int w = __builtin_amdgcn_readfirstlane(threadIdx.x >> 6);
  const int bid = blockIdx.x;
  const int tile = (bid & 7) * 32 + (bid >> 3);
  const int st = tile >> 3, dt = tile & 7;
  f32x16 acc[2][2] = {};
  const unsigned rs = L * 2;
  const int row = lane >> 3, ch = lane & 7;
  const unsigned wv0 = row * rs + ((ch ^ (((row >> 1) & 1) << 2)) << 4);
  const unsigned wv1 = wv0 + 8 * rs;
  const char* wbase = w2 + (size_t)(w * 16) * rs + st * 128;
  const char* abase = ws + WS_UF + ((size_t)dt * 112 + w) * 2048;
  const int h = lane >> 5, blk = (lane >> 4) & 1, q = (lane & 15) >> 2, p = lane & 3;
  const unsigned rd0 = (8 * h + q) * 128 + 64 * (q >> 1) + 32 * blk + 8 * p;
  const unsigned sbase = (unsigned)(size_t)(LAS char*)smem;
  gemm_wave<28, RING, PACT>(acc, wbase, 64 * rs, wv0, wv1, abase, lane * 16, sbase + w * RING * 2048, rd0);
  __syncthreads();
  f32x16 r;
  reduce_quadrants(acc, r, smem, w, lane);
  const int mi = w >> 1, ni = w & 1;
  const int s0 = st * 64 + mi * 32 + 4 * h;
  const int d = dt * 64 + ni * 32 + (lane & 31);
  const float* gp = (const float*)(ws + WS_G);
  const u16* at = (const u16*)(ws + WS_AT) + (size_t)d * L;
  __syncthreads();
  u16* tl = (u16*)(smem + w * 4096);
#pragma unroll
  for (int G = 0; G < 4; G++) {
    u32x2 bb = *(const u32x2*)(b2 + s0 + 8 * G);
    f32x4 gv = *(const f32x4*)(gp + s0 + 8 * G);
    u32x2 aa = *(const u32x2*)(at + s0 + 8 * G);
    float bv[4] = {bflo(bb[0]), bfhi(bb[0]), bflo(bb[1]), bfhi(bb[1])};
    float av[4] = {bflo(aa[0]), bfhi(aa[0]), bflo(aa[1]), bfhi(aa[1])};
#pragma unroll
    for (int j = 0; j < 4; j++) {
      float b = rbf(rbf(r[4 * G + j]) + bv[j]);
      float o = rbf(gv[j] * av[j]) + rbf(rbf(1.f - gv[j]) * b);
      tl[(8 * G + 4 * h + j) * 40 + (lane & 31)] = (u16)(pk_bf16(o, 0.f) & 0xffff);
    }
  }
  __builtin_amdgcn_s_waitcnt(0xC07F);
  __builtin_amdgcn_wave_barrier();
#pragma unroll
  for (int it = 0; it < 2; it++) {
    int rr = it * 16 + (lane >> 2), cc = (lane & 3) * 8;
    u32x4 v = *(const u32x4*)(tl + rr * 40 + cc);
    *(u32x4*)(out + (size_t)(st * 64 + mi * 32 + rr) * D + dt * 64 + ni * 32 + cc) = v;
  }
}

DEVI float wave_sum(float v) {
#pragma unroll
  for (int o = 32; o; o >>= 1) v += __shfl_xor(v, o);
  return v;
}
__global__ __launch_bounds__(256) void ln_kernel(const u16* __restrict__ x, const u16* __restrict__ gw, char* __restrict__ ws) {
  __shared__ float st[8][2];
  const int t = threadIdx.x, lane = t & 63, w = t >> 6;
  const int s0 = blockIdx.x * 8;
  unsigned col[8];
#pragma unroll
  for (int r = 0; r < 8; r++) col[r] = *(const unsigned*)(x + (size_t)(s0 + r) * D + 2 * t);
  u32x4 gv = *(const u32x4*)(gw + lane * 8);
#pragma unroll
  for (int rr = 0; rr < 2; rr++) {
    int r = 2 * w + rr;
    u32x4 xv = *(const u32x4*)(x + (size_t)(s0 + r) * D + lane * 8);
    float v[8];
    float sum = 0, gs = 0;
#pragma unroll
    for (int i = 0; i < 4; i++) {
      v[2 * i] = bflo(xv[i]); v[2 * i + 1] = bfhi(xv[i]);
      sum += v[2 * i] + v[2 * i + 1];
      gs += rbf(v[2 * i] * bflo(gv[i])) + rbf(v[2 * i + 1] * bfhi(gv[i]));
    }
    sum = wave_sum(sum); gs = wave_sum(gs);
    float mean = sum * (1.f / D);
    float vs = 0;
#pragma unroll
    for (int i = 0; i < 8; i++) { float dd = v[i] - mean; vs += dd * dd; }
    vs = wave_sum(vs);
    if (lane == 0) {
      float var = rbf(vs * (1.f / D));
      st[r][0] = rbf(mean);
      st[r][1] = rbf(sqrtf(rbf(var + 1e-5f)));
      float z = rbf(gs);
      ((float*)(ws + WS_G))[s0 + r] = rbf(1.f / (1.f + __expf(-z)));
    }
  }
  __syncthreads();
  float h0[8], h1[8];
#pragma unroll
  for (int r = 0; r < 8; r++) {
    float m = st[r][0], den = st[r][1];
    h0[r] = rbf(bflo(col[r]) - m) / den;
    h1[r] = rbf(bfhi(col[r]) - m) / den;
  }
  u32x4 H0, H1, X0, X1;
#pragma unroll
  for (int i = 0; i < 4; i++) {
    H0[i] = pk_bf16(h0[2 * i], h0[2 * i + 1]); H1[i] = pk_bf16(h1[2 * i], h1[2 * i + 1]);
    X0[i] = (col[2 * i] & 0xffff) | (col[2 * i + 1] << 16);
    X1[i] = (col[2 * i] >> 16) | (col[2 * i + 1] & 0xffff0000u);
  }
  const int d = 2 * t;
  const int dt = d >> 6, c = (d >> 5) & 1, kk = s0 >> 4, hh = (s0 >> 3) & 1;
  char* hp = ws + WS_HF + (((size_t)dt * 128 + kk) * 2 + c) * 1024 + ((d & 31) + 32 * hh) * 16;
  *(u32x4*)hp = H0; *(u32x4*)(hp + 16) = H1;
  char* xp = ws + WS_XC + ((size_t)(s0 >> 3) * 512 + d) * 16;
  *(u32x4*)xp = X0; *(u32x4*)(xp + 16) = X1;
}

constexpr int C_PAR = 0, C_KP = 2048, C_KH = 7296, C_KC = 8640, C_U16 = 25280, C_SK = 29888, C_SIZE = 33984 + 64;

DEVI void cexp_tab(float cr, float ci, float l, float& er, float& ei) {
  float mag = __builtin_amdgcn_exp2f(cr * l);
  float rev = __builtin_amdgcn_fractf(ci * l);
  er = mag * __builtin_amdgcn_cosf(rev);
  ei = mag * __builtin_amdgcn_sinf(rev);
}
DEVI void split8(const float (&v)[8], bf16x8& hi, bf16x8& lo) {
  u32x4 h, l;
#pragma unroll
  for (int i = 0; i < 4; i++) {
    unsigned p = pk_bf16(v[2 * i], v[2 * i + 1]);
    h[i] = p;
    l[i] = pk_bf16(v[2 * i] - bflo(p), v[2 * i + 1] - bfhi(p));
  }
  hi = __builtin_bit_cast(bf16x8, h); lo = __builtin_bit_cast(bf16x8, l);
}
DEVI int compact_idx(int l) {
  float f = (float)l;
  unsigned u = __float_as_uint(f);
  u = (u + 0x7FFF + ((u >> 16) & 1)) >> 16;
  return l < 128 ? l : (int)(u - 0x4300) + 128;
}

__global__ __launch_bounds__(256) void ssm_kernel(const u16* __restrict__ Are, const u16* __restrict__ Aim, const u16* __restrict__ Bre,
    const u16* __restrict__ Bim, const u16* __restrict__ Cre, const u16* __restrict__ Cim, const u16* __restrict__ logdt,
    const u16* __restrict__ Dskip, char* __restrict__ ws) {
  extern __shared__ __attribute__((aligned(16))) char smem[];
  const int t = threadIdx.x, lane = t & 63;
  const int w = __builtin_amdgcn_readfirstlane(t >> 6);
  const int chl = w >> 1, hw = w & 1;
  const int d = blockIdx.x * 2 + chl;
  char* cs = smem + chl * C_SIZE;
  {
    int n = hw * 64 + lane;
    float dt = rbf(__expf(bf2f(logdt[d])));
    float ar = dt * bf2f(Are[d * NM + n]), ai = dt * bf2f(Aim[d * NM + n]);
    float br = dt * bf2f(Bre[d * NM + n]), bi = dt * bf2f(Bim[d * NM + n]);
    float cr = bf2f(Cre[d * NM + n]), ci = bf2f(Cim[d * NM + n]);
    f32x4 pv = {ar * 1.4426950408889634f, ai * 0.15915494309189535f, cr * br - ci * bi, cr * bi + ci * br};
    *(f32x4*)(cs + C_PAR + n * 16) = pv;
  }
  u32x4 xr[2];
#pragma unroll
  for (int i = 0; i < 2; i++) {
    int sb = hw * 128 + i * 64 + lane;
    xr[i] = *(const u32x4*)(ws + WS_XC + ((size_t)sb * 512 + d) * 16);
  }
  __builtin_amdgcn_s_waitcnt(0xC07F);
  __builtin_amdgcn_wave_barrier();
  const int r = lane & 15, g = lane >> 4;
  float hi0 = (float)((r & 7) + 128 * (r >> 3));
  float hi1 = r < 8 ? (float)(256 + 2 * (r & 3) + 128 * ((r >> 2) & 1)) : (float)(512 + 4 * (r & 1) + 128 * ((r >> 1) & 3));
  float hi2 = r < 8 ? (float)(1024 + 128 * r) : 2048.f;
  float lo = (float)(8 * r);
  f32x4 kacc[3] = {};
#pragma unroll 1
  for (int ks = 0; ks < 4; ks++) {
    f32x4 pv[4];
#pragma unroll
    for (int i = 0; i < 4; i++) pv[i] = *(const f32x4*)(cs + C_PAR + (hw * 64 + ks * 16 + 4 * g + i) * 16);
    float v[8];
    bf16x8 bh, bl;
#pragma unroll
    for (int i = 0; i < 4; i++) cexp_tab(pv[i][0], pv[i][1], lo, v[2 * i], v[2 * i + 1]);
    split8(v, bh, bl);
#pragma unroll
    for (int T = 0; T < 3; T++) {
      float hv = T == 0 ? hi0 : T == 1 ? hi1 : hi2;
#pragma unroll
      for (int i = 0; i < 4; i++) {
        float er, ei;
        cexp_tab(pv[i][0], pv[i][1], hv, er, ei);
        v[2 * i] = pv[i][2] * er - pv[i][3] * ei;
        v[2 * i + 1] = -(pv[i][2] * ei + pv[i][3] * er);
      }
      bf16x8 ah, al;
      split8(v, ah, al);
      kacc[T] = __builtin_amdgcn_mfma_f32_16x16x32_bf16(ah, bh, kacc[T], 0, 0, 0);
      kacc[T] = __builtin_amdgcn_mfma_f32_16x16x32_bf16(ah, bl, kacc[T], 0, 0, 0);
      kacc[T] = __builtin_amdgcn_mfma_f32_16x16x32_bf16(al, bh, kacc[T], 0, 0, 0);
    }
  }
  {
    float* kp = (float*)(cs + C_KP) + hw * 656;
    const int b = r;
#pragma unroll
    for (int reg = 0; reg < 4; reg++) {
      int row = 4 * g + reg;
      kp[(row & 7) + 128 * (row >> 3) + 8 * b] = kacc[0][reg];
      int i1 = row < 8 ? 256 + (row & 3) + 64 * ((row >> 2) & 1) + 4 * b : 384 + (row & 1) + 32 * ((row >> 1) & 3) + 2 * b;
      kp[i1] = kacc[1][reg];
      if (row < 8) kp[512 + 16 * row + b] = kacc[2][reg];
      else if (row == 8 && b == 0) kp[640] = kacc[2][reg];
    }
  }
  unsigned umax = 0;
#pragma unroll
  for (int i = 0; i < 2; i++)
#pragma unroll
    for (int j = 0; j < 4; j++) {
      umax = max(umax, (xr[i][j] & 0x7fff)); umax = max(umax, (xr[i][j] >> 16) & 0x7fff);
    }
#pragma unroll
  for (int o = 32; o; o >>= 1) umax = max(umax, (unsigned)__shfl_xor((int)umax, o));
  unsigned* red = (unsigned*)(cs + C_SIZE - 64);
  if (lane == 0) red[hw] = umax;
  __syncthreads();
  umax = max(red[0], red[1]);
  int eu = (int)(umax >> 7) - 127;
  int su = umax ? 14 - eu : 0;
  float kv[6];
  float kmax = 0;
  {
    const float* kp = (const float*)(cs + C_KP);
#pragma unroll
    for (int i = 0; i < 6; i++) {
      int idx = (hw * 6 + i) * 64 + lane;
      kv[i] = idx < 641 ? kp[idx] + kp[656 + idx] : 0.f;
      kmax = fmaxf(kmax, fabsf(kv[i]));
    }
#pragma unroll
    for (int o = 32; o; o >>= 1) kmax = fmaxf(kmax, __shfl_xor(kmax, o));
    if (lane == 0) red[2 + hw] = __float_as_uint(kmax);
  }
  {
    float dsk = bf2f(Dskip[d]);
    u16* u16p = (u16*)(cs + C_U16);
    u16* skp = (u16*)(cs + C_SK);
    if (hw == 0 && lane < 32) *(u32x4*)(u16p + lane * 8) = u32x4{0, 0, 0, 0};
#pragma unroll
    for (int i = 0; i < 2; i++) {
      int sb = hw * 128 + i * 64 + lane;
      u32x4 o, sk;
#pragma unroll
      for (int j = 0; j < 4; j++) {
        float a = bflo(xr[i][j]), b = bfhi(xr[i][j]);
        o[j] = __builtin_bit_cast(unsigned, __builtin_amdgcn_cvt_pkrtz(__builtin_amdgcn_ldexpf(a, su), __builtin_amdgcn_ldexpf(b, su)));
        sk[j] = pk_bf16(a * dsk, b * dsk);
      }
      *(u32x4*)(u16p + 256 + sb * 8) = o;
      *(u32x4*)(skp + sb * 8) = sk;
    }
  }
  __syncthreads();
  unsigned kmb = max(red[2], red[3]);
  int ek = (int)(kmb >> 23) - 127;
  int sk_ = kmb ? 14 - ek : 0;
  {
    u16* kh = (u16*)(cs + C_KH);
#pragma unroll
    for (int i = 0; i < 6; i++) {
      int idx = (hw * 6 + i) * 64 + lane;
      _Float16 hv = (_Float16)__builtin_amdgcn_ldexpf(kv[i], sk_);
      if (idx < 641) kh[idx] = __builtin_bit_cast(u16, hv);
    }
  }
  __syncthreads();
  {
    const u16* kh = (const u16*)(cs + C_KH);
#pragma unroll 1
    for (int ci = hw * 64 + lane; ci < 260; ci += 128) {
      int X = 8 * ci - 16;
      unsigned kvv[11];
#pragma unroll
      for (int j = 0; j < 11; j++) {
        int l = X - 3 + j;
        kvv[j] = (l >= 0 && l < L) ? kh[compact_idx(l)] : 0u;
      }
#pragma unroll
      for (int q = 0; q < 4; q++) {
        u32x4 o;
#pragma unroll
        for (int e = 0; e < 4; e++) o[e] = kvv[3 - q + 2 * e] | (kvv[3 - q + 2 * e + 1] << 16);
        *(u32x4*)(cs + C_KC + q * 4160 + ci * 16) = o;
      }
    }
  }
  __syncthreads();
  f32x4 yacc[8] = {};
  {
    const int qq = (lane & 15) >> 2, pp = lane & 3;
    const unsigned cb = (unsigned)(size_t)(LAS char*)cs;
    const unsigned aaddr = cb + C_KC + qq * 4160 + (16 - 8 * g + 4 * pp + 16) * 2;
    const unsigned baddr = cb + C_U16 + (256 - 16 + 16 * r + 8 * g) * 2;
#pragma unroll 1
    for (int rho = hw * 4; rho < hw * 4 + 4; rho++) {
      f16x8 af[8];
#pragma unroll
      for (int al = 0; al < 8; al++) {
        unsigned ad = aaddr + (rho + 8 * al) * 64;
        s16x4 a0 = tr_read(ad), a1 = tr_read(ad - 8);
        af[al] = __builtin_bit_cast(f16x8, __builtin_shufflevector(a0, a1, 0, 1, 2, 3, 4, 5, 6, 7));
      }
#pragma unroll
      for (int mu = 0; mu < 8; mu++) {
        f16x8 bf = *(const LAS f16x8*)(size_t)(baddr + (8 * mu - rho) * 64);
#pragma unroll
        for (int al = 0; al + mu < 8; al++)
          yacc[mu + al] = __builtin_amdgcn_mfma_f32_16x16x32_f16(af[al], bf, yacc[mu + al], 0, 0, 0);
      }
    }
  }
  {
    f32x4* ex = (f32x4*)(cs + C_KP);
    ex = (f32x4*)(cs + C_PAR);
#pragma unroll
    for (int i = 0; i < 4; i++) ex[(hw * 4 + i) * 64 + lane] = yacc[(1 - hw) * 4 + i];
    __syncthreads();
    const u16* skp = (const u16*)(cs + C_SK);
    u16* at = (u16*)(ws + WS_AT) + (size_t)d * L;
#pragma unroll
    for (int i = 0; i < 4; i++) {
      int ti = hw * 4 + i;
      f32x4 o = ex[((1 - hw) * 4 + i) * 64 + lane];
      f32x4 me = yacc[ti];
      int tt = 256 * ti + 16 * r + 4 * g;
      u32x2 sk = *(const u32x2*)(skp + tt);
      float y0 = rbf(__builtin_amdgcn_ldexpf(me[0] + o[0], -(su + sk_))), y1 = rbf(__builtin_amdgcn_ldexpf(me[1] + o[1], -(su + sk_)));
      float y2 = rbf(__builtin_amdgcn_ldexpf(me[2] + o[2], -(su + sk_))), y3 = rbf(__builtin_amdgcn_ldexpf(me[3] + o[3], -(su + sk_)));
      u32x2 res = {pk_bf16(y0 + bflo(sk[0]), y1 + bfhi(sk[0])), pk_bf16(y2 + bflo(sk[1]), y3 + bfhi(sk[1]))};
      *(u32x2*)(at + tt) = res;
    }
  }
}

extern "C" void kernel_launch(void* const* d_in, const int* in_sizes, int n_in,
                              void* d_out, int out_size, void* d_ws, size_t ws_size,
                              hipStream_t stream) {
  char* ws = (char*)d_ws;
  static bool attr = false;
  if (!attr) {
    hipFuncSetAttribute((const void*)gemm1_kernel, hipFuncAttributeMaxDynamicSharedMemorySize, 4 * RING * 2048);
    hipFuncSetAttribute((const void*)gemm2_kernel, hipFuncAttributeMaxDynamicSharedMemorySize, 4 * RING * 2048);
    hipFuncSetAttribute((const void*)ssm_kernel, hipFuncAttributeMaxDynamicSharedMemorySize, 2 * C_SIZE);
    attr = true;
  }
  ln_kernel<<<256, 256, 0, stream>>>((const u16*)d_in[0], (const u16*)d_in[14], ws);
  ssm_kernel<<<256, 256, 2 * C_SIZE, stream>>>((const u16*)d_in[1], (const u16*)d_in[2], (const u16*)d_in[3], (const u16*)d_in[4],
      (const u16*)d_in[5], (const u16*)d_in[6], (const u16*)d_in[7], (const u16*)d_in[8], ws);
  gemm1_kernel<<<224, 256, 4 * RING * 2048, stream>>>((const char*)d_in[10], (const u16*)d_in[11], ws);
  gemm2_kernel<<<256, 256, 4 * RING * 2048, stream>>>((const char*)d_in[12], (const u16*)d_in[13], ws, (u16*)d_out);
}
```

```cpp
#include <hip/hip_runtime.h>
#include <stdint.h>
#include <type_traits>

typedef unsigned short u16;
typedef unsigned long long u64;
typedef short s16x4 __attribute__((ext_vector_type(4)));
typedef __bf16 bf16x8 __attribute__((ext_vector_type(8)));
typedef _Float16 f16x8 __attribute__((ext_vector_type(8)));
typedef float f32x2 __attribute__((ext_vector_type(2)));
typedef float f32x4 __attribute__((ext_vector_type(4)));
typedef float f32x16 __attribute__((ext_vector_type(16)));
typedef unsigned u32x2 __attribute__((ext_vector_type(2)));
typedef unsigned u32x4 __attribute__((ext_vector_type(4)));
#define LAS __attribute__((address_space(3)))
#define GAS __attribute__((address_space(1)))
#if defined(__HIP_DEVICE_COMPILE__)
#define NOPK __attribute__((target("no-packed-fp32-ops")))
#else
#define NOPK
#endif
#define DEVI __device__ __forceinline__ NOPK

constexpr int L = 2048, D = 512, NM = 128, F = 1792;

DEVI float u2f(unsigned v) { return __builtin_bit_cast(float, v); }
DEVI unsigned f2u(float v) { return __builtin_bit_cast(unsigned, v); }
DEVI float bflo(unsigned v) { return u2f(v << 16); }
DEVI float bfhi(unsigned v) { return u2f(v & 0xffff0000u); }
DEVI unsigned pk_bf16(float lo, float hi) {
  typedef __bf16 b2 __attribute__((ext_vector_type(2)));
  f32x2 v = {lo, hi};
  b2 r = __builtin_convertvector(v, b2);
  return __builtin_bit_cast(unsigned, r);
}
DEVI float rbf(float f) { return bflo(pk_bf16(f, 0.f)); }

constexpr size_t WS_HF = 0;
constexpr size_t WS_XC = 2u << 20;
constexpr size_t WS_UF = 4u << 20;
constexpr size_t WS_AT = 6u << 20;
constexpr size_t WS_G  = 8u << 20;
constexpr size_t WS_F1 = (8u << 20) + 65536;
constexpr size_t WS_F2 = WS_F1 + 16384;
constexpr size_t WS_F3 = WS_F2 + 16384;
constexpr size_t WS_GEN = WS_F3 + 16384;
constexpr size_t WS_DBG = 16u << 20;

#define CFG_NWR 10
#define CFG_PACT 8
#define CFG_EARLY 8
constexpr int RING = 12, PACT = CFG_PACT, NWR = CFG_NWR, EARLY = CFG_EARLY;
constexpr int LDS_RING = 0;
constexpr int LDS_AUX = 4 * RING * 2048;
constexpr int C_PAR = 0, C_SK = 2048, C_KH = 7296, C_KC = 8640, C_U16 = 25280, C_SIZE = 29888;
constexpr int LDS_CTL = LDS_AUX + 2 * C_SIZE;
constexpr int LDS_LNST = LDS_CTL + 256;
constexpr int LDS_TOTAL = 163840;
enum { CW_TAGV = 0, CW_TAG = 1, CW_F1 = 2, CW_GB = 3, CW_AB = 4, CW_P0 = 5, CW_P1 = 6, CW_KG = 7, CW_F2 = 8 };

template <int I, int N, class Fn>
DEVI void static_for(Fn&& f) {
  if constexpr (I < N) { f(std::integral_constant<int, I>{}); static_for<I + 1, N>(f); }
}
DEVI void dma16(unsigned lds_addr, const void* sbase, unsigned voff) {
  unsigned keep;
  asm volatile("s_mov_b32 %0, m0\n\ts_mov_b32 m0, %1\n\ts_nop 0\n\tglobal_load_lds_dwordx4 %2, %3\n\ts_mov_b32 m0, %0"
               : "=&s"(keep) : "s"(lds_addr), "v"(voff), "s"(sbase) : "memory");
}
template <int OFF>
DEVI void gload16_sc1(u32x4& dst, const void* sbase, unsigned voff) {
  asm volatile("global_load_dwordx4 %0, %1, %2 offset:%3 sc1" : "=v"(dst) : "v"(voff), "s"(sbase), "i"(OFF) : "memory");
}
template <int N>
DEVI void wait_vm2(u32x4& a, u32x4& b) {
  asm volatile("s_waitcnt vmcnt(%2)" : "+v"(a), "+v"(b) : "i"(N) : "memory");
}
DEVI const char* uni_ptr(const char* p) {
  u64 v = (u64)p;
  unsigned lo = __builtin_amdgcn_readfirstlane((unsigned)v), hi = __builtin_amdgcn_readfirstlane((unsigned)(v >> 32));
  return (const char*)(((u64)hi << 32) | lo);
}
DEVI void wait_vm0() { asm volatile("s_waitcnt vmcnt(0)" ::: "memory"); }
DEVI void wait_lgkm0() { asm volatile("s_waitcnt lgkmcnt(0)" ::: "memory"); }
DEVI s16x4 tr_read(unsigned addr) {
  return __builtin_amdgcn_ds_read_tr16_b64_v4i16((LAS s16x4*)(size_t)addr);
}
DEVI __amdgpu_buffer_rsrc_t mk_rsrc(const void* p, unsigned bytes) {
  return __builtin_amdgcn_make_buffer_rsrc((void*)p, 0, bytes, 0x00020000);
}
DEVI u32x4 ld16_sc1(__amdgpu_buffer_rsrc_t r, unsigned off) { return __builtin_amdgcn_raw_buffer_load_b128(r, off, 0, 16); }
DEVI u32x4 ld16_x(__amdgpu_buffer_rsrc_t r, unsigned off) { return __builtin_amdgcn_raw_buffer_load_b128(r, off, 0, 16); }
DEVI u32x2 ld8_sc1(__amdgpu_buffer_rsrc_t r, unsigned off) { return __builtin_amdgcn_raw_buffer_load_b64(r, off, 0, 16); }
DEVI void st16_sc1(__amdgpu_buffer_rsrc_t r, unsigned off, u32x4 v) { __builtin_amdgcn_raw_buffer_store_b128(v, r, off, 0, 16); }
DEVI void st8_sc1(__amdgpu_buffer_rsrc_t r, unsigned off, u32x2 v) { __builtin_amdgcn_raw_buffer_store_b64(v, r, off, 0, 16); }
DEVI void st4_sc1(__amdgpu_buffer_rsrc_t r, unsigned off, unsigned v) { __builtin_amdgcn_raw_buffer_store_b32(v, r, off, 0, 16); }

template <int CTRL, int RM>
DEVI float dpp_f(float v) { return u2f(__builtin_amdgcn_update_dpp(0, f2u(v), CTRL, RM, 0xf, true)); }
template <int CTRL, int RM>
DEVI unsigned dpp_u(unsigned v) { return __builtin_amdgcn_update_dpp(0, v, CTRL, RM, 0xf, true); }
DEVI float wave_sum(float v) {
  v += dpp_f<0x111, 0xf>(v); v += dpp_f<0x112, 0xf>(v); v += dpp_f<0x114, 0xf>(v); v += dpp_f<0x118, 0xf>(v);
  v += dpp_f<0x142, 0xa>(v); v += dpp_f<0x143, 0xc>(v);
  return u2f(__builtin_amdgcn_readlane(f2u(v), 63));
}
DEVI unsigned wave_umax(unsigned v) {
  v = max(v, dpp_u<0x111, 0xf>(v)); v = max(v, dpp_u<0x112, 0xf>(v)); v = max(v, dpp_u<0x114, 0xf>(v)); v = max(v, dpp_u<0x118, 0xf>(v));
  v = max(v, dpp_u<0x142, 0xa>(v)); v = max(v, dpp_u<0x143, 0xc>(v));
  return __builtin_amdgcn_readlane(v, 63);
}

DEVI unsigned lds_ld(unsigned addr) {
  unsigned v;
  asm volatile("ds_read_b32 %0, %1\n\ts_waitcnt lgkmcnt(0)" : "=v"(v) : "v"(addr) : "memory");
  return __builtin_amdgcn_readfirstlane(v);
}
DEVI void lds_st(unsigned addr, unsigned v) { asm volatile("ds_write_b32 %0, %1" :: "v"(addr), "v"(v) : "memory"); }
DEVI void lds_add1(unsigned addr) { asm volatile("ds_add_u32 %0, %1" :: "v"(addr), "v"(1u) : "memory"); }
constexpr unsigned SPIN_CAP = 1u << 22;
template <int SLP = 1>
DEVI void lds_wait_ge(unsigned addr, unsigned target) {
  for (unsigned sp = 0; sp < SPIN_CAP; sp++) {
    if (lds_ld(addr) >= target) break;
    __builtin_amdgcn_s_sleep(SLP);
  }
}
DEVI void group_barrier(unsigned addr, unsigned target, int lane) {
  wait_lgkm0();
  if (lane == 0) lds_add1(addr);
  lds_wait_ge(addr, target);
}

#define STAMP_DECL
#define STAMP_AT(i)
#define STAMP_OUT(kid, n)

DEVI void poll_flags(const u64* flags, int n, u64 tag, int lane) {
  const GAS u64* f = (const GAS u64*)flags;
  for (unsigned sp = 0; sp < (1u << 20); sp++) {
    bool ok = true;
#pragma unroll
    for (int j = 0; j < 4; j++) {
      int i = j * 64 + lane;
      if (j * 64 < n) {
        u64 v = (i < n) ? __hip_atomic_load(f + i, __ATOMIC_RELAXED, __HIP_MEMORY_SCOPE_AGENT) : tag;
        ok &= (v == tag);
      }
    }
    if ((__builtin_amdgcn_ballot_w64(ok) == ~0ull)) break;
    __builtin_amdgcn_s_sleep(2);
  }
}

struct WStream { const char* base; unsigned step, v0, v1; int rot, ns; };
DEVI unsigned rot_step(int step, int rot, int ns) { int k = step + rot; return (unsigned)(k >= ns ? k - ns : k); }
DEVI void issue_w(const WStream& ws_, int step, unsigned slot) {
  const char* b = ws_.base + (size_t)(rot_step(step, ws_.rot, ws_.ns) * ws_.step);
  dma16(slot, b, ws_.v0);
  dma16(slot + 1024, b, ws_.v1);
}
template <int NS> constexpr int pos2step(int i) { return i < RING ? NWR + i : (i < RING + NWR ? i - RING : i); }
template <int NS> constexpr int n_rem() { return NS - NWR - RING; }
template <int NS> constexpr int gemm_ops_at(int p) { return (p < n_rem<NS>() ? 2 : 0) + ((p + PACT < NS) ? 2 : 0); }
template <int NS> constexpr int gemm_cnt(int i) {
  int cnt = 0;
  int last = i - 2;
  if (i < PACT) { cnt = 2 * (PACT - 1 - i); for (int s = 0; s <= last; s++) cnt += gemm_ops_at<NS>(s); }
  else { for (int s = i - PACT + 1; s <= last; s++) cnt += gemm_ops_at<NS>(s); }
  return cnt;
}
template <int NS> constexpr int gemm_cnt2(int i) {
  int cnt = 0;
  int first = 0;
  if (i < PACT) { cnt = 2 * (PACT - 1 - i); first = 0; }
  else first = i - PACT + 1;
  for (int s = first; s <= i - 2; s++) cnt += gemm_ops_at<NS>(s);
  if (i - 1 >= first && i - 1 < n_rem<NS>()) cnt += 2;
  return cnt;
}
typedef s16x4 WFrag[4];
template <int OFF>
DEVI void tr_asm(s16x4& d, unsigned addr) {
  asm volatile("ds_read_b64_tr_b16 %0, %1 offset:%2" : "=v"(d) : "v"(addr), "i"(OFF) : "memory");
}
template <int SLOT>
DEVI void read_w(WFrag& n, unsigned a0, unsigned a1) {
  tr_asm<SLOT * 2048>(n[0], a0); tr_asm<SLOT * 2048 + 512>(n[1], a0);
  tr_asm<SLOT * 2048>(n[2], a1); tr_asm<SLOT * 2048 + 512>(n[3], a1);
}
template <int N>
DEVI void wait_lgkm_w(WFrag& f) {
  asm volatile("s_waitcnt lgkmcnt(%4)" : "+v"(f[0]), "+v"(f[1]), "+v"(f[2]), "+v"(f[3]) : "i"(N) : "memory");
}
DEVI void preload_regs(WFrag (&WR)[NWR], const WStream& wc, unsigned ring, unsigned a0, unsigned a1) {
  static_for<0, NWR>([&](auto ic) NOPK __attribute__((always_inline)) {
    constexpr int j = ic.value;
    read_w<j>(WR[j], a0, a1);
    if constexpr ((j % 3) == 2 || j == NWR - 1) {
      wait_lgkm_w<0>(WR[j]);
      if constexpr (j % 3 >= 1) wait_lgkm_w<0>(WR[j - 1]);
      if constexpr (j % 3 >= 2) wait_lgkm_w<0>(WR[j - 2]);
    }
  });
  static_for<0, NWR>([&](auto ic) NOPK __attribute__((always_inline)) { constexpr int j = ic.value; issue_w(wc, RING + j, ring + j * 2048); });
}
#define LST_ARG
#define LST_PASS
template <int NS>
DEVI void gemm_loop(f32x16 (&acc)[2][2], WFrag (&WR)[NWR], const WStream& wc, const char* abase, unsigned lane16, unsigned ring, unsigned a0_, unsigned a1_ LST_ARG) {
  u32x4 act[PACT][2];
  static_for<0, PACT>([&](auto ic) NOPK __attribute__((always_inline)) {
    constexpr int i = ic.value;
    constexpr int sp = pos2step<NS>(i);
    const char* ab = abase + (size_t)(rot_step(sp, wc.rot, NS) * 8192u);
    gload16_sc1<0>(act[i][0], ab, lane16);
    gload16_sc1<1024>(act[i][1], ab, lane16);
  });
  WFrag wt[2];
  wait_vm2<gemm_cnt<NS>(0)>(act[0][0], act[0][1]);
  { constexpr int sp0 = pos2step<NS>(0); read_w<sp0 % RING>(wt[0], a0_, a1_); }
#define SB() __builtin_amdgcn_sched_barrier(0)
  static_for<0, NS>([&](auto ic) NOPK __attribute__((always_inline)) {
    constexpr int i = ic.value;
    constexpr int st = pos2step<NS>(i);
    constexpr bool in_reg = st < NWR;
    constexpr int sn = pos2step<NS>(i + 1 < NS ? i + 1 : 0);
    constexpr bool next_lds = (i + 1 < NS) && (sn >= NWR);
    constexpr int nrem = n_rem<NS>();
    u32x4& a0 = act[i % PACT][0];
    u32x4& a1 = act[i % PACT][1];
    WFrag& c = in_reg ? WR[in_reg ? st : 0] : wt[i & 1];
    if constexpr (!in_reg) wait_lgkm_w<0>(c);
    bf16x8 wf0 = __builtin_bit_cast(bf16x8, __builtin_shufflevector(c[0], c[1], 0, 1, 2, 3, 4, 5, 6, 7));
    bf16x8 wf1 = __builtin_bit_cast(bf16x8, __builtin_shufflevector(c[2], c[3], 0, 1, 2, 3, 4, 5, 6, 7));
    bf16x8 h0 = __builtin_bit_cast(bf16x8, a0), h1 = __builtin_bit_cast(bf16x8, a1);
    SB();
    acc[0][0] = __builtin_amdgcn_mfma_f32_32x32x16_bf16(wf0, h0, acc[0][0], 0, 0, 0);
    SB();
    if constexpr (i < nrem) issue_w(wc, NWR + RING + i, ring + (st % RING) * 2048);
    SB();
    acc[1][0] = __builtin_amdgcn_mfma_f32_32x32x16_bf16(wf1, h0, acc[1][0], 0, 0, 0);
    SB();
    if constexpr (i + 1 < NS) {
      wait_vm2<gemm_cnt2<NS>(i + 1)>(act[(i + 1) % PACT][0], act[(i + 1) % PACT][1]);
      if constexpr (next_lds) read_w<sn % RING>(wt[(i + 1) & 1], a0_, a1_);
    }
    SB();
    acc[0][1] = __builtin_amdgcn_mfma_f32_32x32x16_bf16(wf0, h1, acc[0][1], 0, 0, 0);
    SB();
    constexpr int sp = pos2step<NS>(i + PACT < NS ? i + PACT : 0);
    const char* ab = abase + (size_t)(rot_step(sp, wc.rot, NS) * 8192u);
    if constexpr (i + PACT < NS) gload16_sc1<0>(a0, ab, lane16);
    SB();
    acc[1][1] = __builtin_amdgcn_mfma_f32_32x32x16_bf16(wf1, h1, acc[1][1], 0, 0, 0);
    SB();
    if constexpr (i + PACT < NS) gload16_sc1<1024>(a1, ab, lane16);
    SB();
  });
}

DEVI void reduce_quadrants(f32x16 (&acc)[2][2], f32x16& res, char* smem, int xoff, int w, int lane, unsigned gb_addr, unsigned gb_target) {
#pragma unroll
  for (int q = 0; q < 4; q++) {
    if (q != w) {
      int sl = ((q - w) & 3) - 1;
      f32x4* p = (f32x4*)(smem + w * (RING * 2048) + xoff + sl * 4096 + lane * 16);
      const f32x16& a = acc[q >> 1][q & 1];
#pragma unroll
      for (int g = 0; g < 4; g++) p[g * 64] = f32x4{a[4 * g], a[4 * g + 1], a[4 * g + 2], a[4 * g + 3]};
    }
  }
  group_barrier(gb_addr, gb_target, lane);
  res = (w == 0) ? acc[0][0] : (w == 1) ? acc[0][1] : (w == 2) ? acc[1][0] : acc[1][1];
#pragma unroll
  for (int j = 1; j < 4; j++) {
    int src = (w - j) & 3;
    const f32x4* p = (const f32x4*)(smem + src * (RING * 2048) + xoff + (j - 1) * 4096 + lane * 16);
#pragma unroll
    for (int g = 0; g < 4; g++) {
      f32x4 v = p[g * 64];
      res[4 * g] += v[0]; res[4 * g + 1] += v[1]; res[4 * g + 2] += v[2]; res[4 * g + 3] += v[3];
    }
  }
}

DEVI void cexp_tab(float cr, float ci, float l, float& er, float& ei) {
  float mag = __builtin_amdgcn_exp2f(cr * l);
  float rev = __builtin_amdgcn_fractf(ci * l);
  er = mag * __builtin_amdgcn_cosf(rev);
  ei = mag * __builtin_amdgcn_sinf(rev);
}
DEVI void split8(const float (&v)[8], bf16x8& hi, bf16x8& lo) {
  u32x4 h, l;
#pragma unroll
  for (int i = 0; i < 4; i++) {
    unsigned p = pk_bf16(v[2 * i], v[2 * i + 1]);
    h[i] = p;
    l[i] = pk_bf16(v[2 * i] - bflo(p), v[2 * i + 1] - bfhi(p));
  }
  hi = __builtin_bit_cast(bf16x8, h); lo = __builtin_bit_cast(bf16x8, l);
}
DEVI int compact_idx(int l) {
  float f = (float)l;
  unsigned u = f2u(f);
  u = (u + 0x7FFF + ((u >> 16) & 1)) >> 16;
  return l < 128 ? l : (int)(u - 0x4300) + 128;
}

struct Params {
  const u16 *x, *Are, *Aim, *Bre, *Bim, *Cre, *Cim, *logdt, *Dskip, *b1, *b2, *gw;
  const char *w1, *w2;
  char* ws;
  u16* out;
};

DEVI void post_flag(char* ws, size_t base, int idx, u64 tag, int lane) {
  if (lane < 8) __hip_atomic_store((GAS u64*)(ws + base) + lane * 256 + idx, tag, __ATOMIC_RELAXED, __HIP_MEMORY_SCOPE_AGENT);
}
#define ACQ()
DEVI void wg_barrier() { wait_lgkm0(); __builtin_amdgcn_s_barrier(); }
DEVI void gl16(u32x4& dst, const void* sbase, unsigned voff) {
  asm volatile("global_load_dwordx4 %0, %1, %2" : "=v"(dst) : "v"(voff), "s"(sbase) : "memory");
}
template <int OFF>
DEVI void glu16(unsigned& dst, const void* sbase, unsigned voff) {
  asm volatile("global_load_ushort %0, %1, %2 offset:%3" : "=v"(dst) : "v"(voff), "s"(sbase), "i"(OFF) : "memory");
}
template <int OFF>
DEVI void gl4o(unsigned& dst, const void* sbase, unsigned voff) {
  asm volatile("global_load_dword %0, %1, %2 offset:%3" : "=v"(dst) : "v"(voff), "s"(sbase), "i"(OFF) : "memory");
}
DEVI void gl4_sc1(unsigned& dst, const void* sbase, unsigned voff) {
  asm volatile("global_load_dword %0, %1, %2 sc1" : "=v"(dst) : "v"(voff), "s"(sbase) : "memory");
}

DEVI unsigned __shfl_down_16(unsigned v) { return (unsigned)__builtin_amdgcn_ds_bpermute((int)(((threadIdx.x & 63) + 16) & 63) * 4, (int)v); }
DEVI unsigned __shfl_down_32(unsigned v) { return (unsigned)__builtin_amdgcn_ds_bpermute((int)(((threadIdx.x & 63) + 32) & 63) * 4, (int)v); }
DEVI void kgen_unit(f32x4 (&kacc)[3], char* par, int lane, unsigned pA, unsigned pB, unsigned pC, float dt) {
  {
    float ar = dt * bflo(pA), ai = dt * bfhi(pA), br = dt * bflo(pB), bi = dt * bfhi(pB), cr = bflo(pC), ci = bfhi(pC);
    f32x4 pv = {ar * 1.4426950408889634f, ai * 0.15915494309189535f, cr * br - ci * bi, cr * bi + ci * br};
    if (lane < 16) *(f32x4*)(par + lane * 16) = pv;
  }
  wait_lgkm0();
  __builtin_amdgcn_wave_barrier();
  const int r = lane & 15, g = lane >> 4;
  float hi0 = (float)((r & 7) + 128 * (r >> 3));
  float hi1 = r < 8 ? (float)(256 + 2 * (r & 3) + 128 * ((r >> 2) & 1)) : (float)(512 + 4 * (r & 1) + 128 * ((r >> 1) & 3));
  float hi2 = r < 8 ? (float)(1024 + 128 * r) : 2048.f;
  float lo = (float)(8 * r);
  f32x4 pv[4];
#pragma unroll
  for (int i = 0; i < 4; i++) pv[i] = *(const f32x4*)(par + (4 * g + i) * 16);
  float v[8];
  bf16x8 bh, bl;
#pragma unroll
  for (int i = 0; i < 4; i++) cexp_tab(pv[i][0], pv[i][1], lo, v[2 * i], v[2 * i + 1]);
  split8(v, bh, bl);
#pragma unroll
  for (int T = 0; T < 3; T++) {
    float hv = T == 0 ? hi0 : T == 1 ? hi1 : hi2;
#pragma unroll
    for (int i = 0; i < 4; i++) {
      float er, ei;
      cexp_tab(pv[i][0], pv[i][1], hv, er, ei);
      v[2 * i] = pv[i][2] * er - pv[i][3] * ei;
      v[2 * i + 1] = -(pv[i][2] * ei + pv[i][3] * er);
    }
    bf16x8 ah, al;
    split8(v, ah, al);
    kacc[T] = __builtin_amdgcn_mfma_f32_16x16x32_bf16(ah, bh, kacc[T], 0, 0, 0);
    kacc[T] = __builtin_amdgcn_mfma_f32_16x16x32_bf16(ah, bl, kacc[T], 0, 0, 0);
    kacc[T] = __builtin_amdgcn_mfma_f32_16x16x32_bf16(al, bh, kacc[T], 0, 0, 0);
  }
  wait_lgkm0();
}
DEVI void kgen_store(const f32x4 (&kacc)[3], float* kp, int lane) {
  const int b = lane & 15, g = lane >> 4;
#pragma unroll
  for (int reg = 0; reg < 4; reg++) {
    int row = 4 * g + reg;
    kp[(row & 7) + 128 * (row >> 3) + 8 * b] = kacc[0][reg];
    int i1 = row < 8 ? 256 + (row & 3) + 64 * ((row >> 2) & 1) + 4 * b : 384 + (row & 1) + 32 * ((row >> 1) & 3) + 2 * b;
    kp[i1] = kacc[1][reg];
    if (row < 8) kp[512 + 16 * row + b] = kacc[2][reg];
    else if (row == 8 && b == 0) kp[640] = kacc[2][reg];
  }
}

NOPK __global__ __launch_bounds__(512, 2) void mega_kernel(Params P) {
  extern __shared__ __attribute__((aligned(16))) char smem[];
  char* ws = P.ws;
  const int tid = threadIdx.x;
  const int lane = tid & 63;
  const int w = __builtin_amdgcn_readfirstlane(tid >> 6);
  const int bid = blockIdx.x;
  const unsigned sbase = (unsigned)(size_t)(LAS char*)smem;
  const unsigned ctl = sbase + LDS_CTL;
  STAMP_DECL
  STAMP_AT(0);
  const int xcd = bid & 7, xi = bid >> 3;
  const int s0 = bid * 8;
  const bool isg = w < 4;
  const bool has1 = xi < 28;
  const int dt1 = (xcd & 1) * 4 + (xi & 3), ft = (xcd >> 1) * 7 + (has1 ? (xi >> 2) : 0);
  const int dt2 = (xcd & 1) * 4 + (xi & 3), st = (xcd >> 1) * 8 + (xi >> 2);
  const int row = lane >> 3, ch8 = lane & 7;
  const unsigned swz = ((ch8 ^ (((row >> 1) & 1) << 2)) << 4);
  WStream W1, W2;
  W1.base = uni_ptr(P.w1 + (size_t)((w & 3) * 16) * (F * 2) + ft * 128); W1.step = 64 * F * 2; W1.v0 = row * (F * 2) + swz; W1.v1 = W1.v0 + 8 * F * 2;
  W2.base = uni_ptr(P.w2 + (size_t)((w & 3) * 16) * (L * 2) + st * 128); W2.step = 64 * L * 2; W2.v0 = row * (L * 2) + swz; W2.v1 = W2.v0 + 8 * L * 2;
  W1.rot = ((xi >> 2) * 32) / 7; W2.rot = ((xi >> 2) * 28) / 8;
  if (!has1) W1.rot = 0;
  W1.ns = 32; W2.ns = 28;
  const unsigned ring = sbase + LDS_RING + (w & 3) * RING * 2048;
  const int kch = (w & 3) >> 1;
  const int cpair = xcd * 32 + xi;
  const int kd = cpair * 2 + kch;
  const int ks0 = isg ? 3 * (w & 1) : 6 + (w & 1);
  u32x4 xv, gv;
  unsigned col[8], gen = 0, pre[7];
  {
    const char* xrow = uni_ptr((const char*)(P.x + (size_t)s0 * D));
    gl16(xv, xrow + w * 1024, lane * 16);
    gl16(gv, P.gw, lane * 16);
    if (w == 4) gl4_sc1(gen, ws + WS_GEN, 0);
    const unsigned po = ((unsigned)kd * NM + min(ks0 * 16 + lane, NM - 1)) * 2;
    glu16<0>(pre[0], P.Are, po); glu16<0>(pre[1], P.Aim, po); glu16<0>(pre[2], P.Bre, po); glu16<0>(pre[3], P.Bim, po);
    glu16<0>(pre[4], P.Cre, po); glu16<0>(pre[5], P.Cim, po); glu16<0>(pre[6], P.logdt, kd * 2);
    static_for<0, 4>([&](auto ic) NOPK __attribute__((always_inline)) { constexpr int r = ic.value; gl4o<r * 1024>(col[r], xrow, (tid & 255) * 4); });
    static_for<0, 4>([&](auto ic) NOPK __attribute__((always_inline)) { constexpr int r = ic.value; gl4o<r * 1024>(col[4 + r], xrow + 4096, (tid & 255) * 4); });
  }
  __builtin_amdgcn_s_barrier();
  if (isg) {
    if (has1) static_for<0, EARLY>([&](auto ic) NOPK __attribute__((always_inline)) { constexpr int i = ic.value; issue_w(W1, i, ring + i * 2048); });
    else static_for<0, EARLY>([&](auto ic) NOPK __attribute__((always_inline)) { constexpr int i = ic.value; issue_w(W2, i, ring + i * 2048); });
  }
  {
    const unsigned isg_u = __builtin_amdgcn_readfirstlane(isg ? 1u : 0u);
    asm volatile("s_cmp_lg_u32 %18, 0\n\ts_cbranch_scc1 1f\n\ts_waitcnt vmcnt(0)\n\ts_branch 2f\n1:\n\ts_waitcnt vmcnt(%19)\n2:"
                 : "+v"(xv), "+v"(gv), "+v"(col[0]), "+v"(col[1]), "+v"(col[2]), "+v"(col[3]), "+v"(col[4]), "+v"(col[5]), "+v"(col[6]), "+v"(col[7]),
                   "+v"(gen), "+v"(pre[0]), "+v"(pre[1]), "+v"(pre[2]), "+v"(pre[3]), "+v"(pre[4]), "+v"(pre[5]), "+v"(pre[6])
                 : "s"(isg_u), "i"(2 * EARLY) : "memory", "scc");
  }
  u64 tag;
  float* stt = (float*)(smem + LDS_LNST);
  {
    if (tid < 32) ((unsigned*)(smem + LDS_CTL))[tid] = 0;
    float v[8];
    float sum = 0, gs = 0;
#pragma unroll
    for (int i = 0; i < 4; i++) {
      v[2 * i] = bflo(xv[i]); v[2 * i + 1] = bfhi(xv[i]);
      sum += v[2 * i] + v[2 * i + 1];
      gs += rbf(v[2 * i] * bflo(gv[i])) + rbf(v[2 * i + 1] * bfhi(gv[i]));
    }
    sum = wave_sum(sum); gs = wave_sum(gs);
    float mean = sum * (1.f / D);
    float vs = 0;
#pragma unroll
    for (int i = 0; i < 8; i++) { float dd = v[i] - mean; vs += dd * dd; }
    vs = wave_sum(vs);
    float var = rbf(vs * (1.f / D));
    float den = rbf(__builtin_amdgcn_sqrtf(rbf(var + 1e-5f)));
    float z = rbf(gs);
    float g = rbf(__builtin_amdgcn_rcpf(1.f + __builtin_amdgcn_exp2f(z * -1.4426950408889634f)));
    if (lane == 0) *(f32x4*)(stt + 4 * w) = f32x4{rbf(mean), den, __builtin_amdgcn_rcpf(den), g};
    if (w == 4 && lane == 0) ((unsigned*)stt)[32] = gen + 1;
  }
  wg_barrier();
  { unsigned t32 = __builtin_amdgcn_readfirstlane(((const unsigned*)stt)[32]); tag = ((u64)t32 << 32) | (unsigned)~t32; }
  const float dtv = rbf(__builtin_amdgcn_exp2f(bflo(pre[6]) * 1.4426950408889634f));
  const unsigned pA = pre[0] | (pre[1] << 16), pB = pre[2] | (pre[3] << 16), pC = pre[4] | (pre[5] << 16);
  char* kcs = smem + LDS_AUX + kch * C_SIZE;
  char* kpar = kcs + C_PAR + ((w >> 2) * 2 + (w & 1)) * 256;
  float* kpp = (float*)(kcs + C_KC) + ((w >> 2) * 2 + (w & 1)) * 656;
  f32x4 kacc[3] = {};
  if (!isg) {
    __builtin_amdgcn_s_setprio(3);
    const int t = tid - 256;
    float h0[8], h1[8];
#pragma unroll
    for (int r = 0; r < 8; r++) {
      f32x4 sv = *(const f32x4*)(stt + 4 * r);
      float n0 = rbf(bflo(col[r]) - sv[0]), n1 = rbf(bfhi(col[r]) - sv[0]);
      float q0 = n0 * sv[2], q1 = n1 * sv[2];
      h0[r] = __builtin_fmaf(__builtin_fmaf(-q0, sv[1], n0), sv[2], q0);
      h1[r] = __builtin_fmaf(__builtin_fmaf(-q1, sv[1], n1), sv[2], q1);
    }
    u32x4 H0, H1, X0, X1;
#pragma unroll
    for (int i = 0; i < 4; i++) {
      H0[i] = pk_bf16(h0[2 * i], h0[2 * i + 1]); H1[i] = pk_bf16(h1[2 * i], h1[2 * i + 1]);
      X0[i] = (col[2 * i] & 0xffff) | (col[2 * i + 1] << 16);
      X1[i] = (col[2 * i] >> 16) | (col[2 * i + 1] & 0xffff0000u);
    }
    const int d = 2 * t;
    const int dt = d >> 6, c = (d >> 5) & 1, kk = s0 >> 4, hh = (s0 >> 3) & 1;
    __amdgpu_buffer_rsrc_t hr = mk_rsrc(ws + WS_HF, 4u << 20);
    unsigned ho = (((unsigned)dt * 128 + kk) * 2 + c) * 1024 + ((d & 31) + 32 * hh) * 16;
    st16_sc1(hr, ho, H0); st16_sc1(hr, ho + 16, H1);
    unsigned xo = (unsigned)WS_XC + ((unsigned)(s0 >> 3) * 512 + d) * 16;
    st16_sc1(hr, xo, X0); st16_sc1(hr, xo + 16, X1);
    if (t < 8) st4_sc1(mk_rsrc(ws + WS_G, 8192), (s0 + t) * 4, f2u(stt[4 * t + 3]));
    wait_vm0();
    group_barrier(ctl + CW_AB * 4, 4, lane);
    if (w == 4) post_flag(ws, WS_F1, bid, tag, lane);
    __builtin_amdgcn_s_setprio(0);
    {
      const char* b2p = uni_ptr(P.w2 + (size_t)bid * 28672 + (w - 4) * 7168);
      const unsigned dst = sbase + LDS_AUX + (w & 2 ? C_SIZE : 0) + C_U16 + (w & 1) * 2048;
      dma16(dst, b2p, lane * 16); dma16(dst + 1024, b2p + 1024, lane * 16);
      dma16(dst, b2p + 2048, lane * 16); dma16(dst + 1024, b2p + 3072, lane * 16);
      dma16(dst, b2p + 4096, lane * 16); dma16(dst + 1024, b2p + 5120, lane * 16);
      dma16(dst, b2p + 6144, lane * 16);
    }
    STAMP_AT(1);
    kgen_unit(kacc, kpar, lane, pA, pB, pC, dtv);
    kgen_store(kacc, kpp, lane);
    wait_lgkm0();
    if (lane == 0) lds_add1(ctl + CW_KG * 4);
    STAMP_AT(2);
  }

  if (isg) {
    const int h = lane >> 5, blk = (lane >> 4) & 1, q = (lane & 15) >> 2, p = lane & 3;
    const unsigned rd0 = (8 * h + q) * 128 + 64 * (q >> 1) + 32 * blk + 8 * p;
    const unsigned ra0 = ring + rd0, ra1 = ring + (rd0 ^ 64);
    const int mi = w >> 1, ni = w & 1;
    unsigned gbt = 0;
    WFrag WR[NWR];
    const int f0 = ft * 64 + mi * 32 + 4 * h;
    u32x2 b1v[4];
#pragma unroll
    for (int G = 0; G < 4; G++) b1v[G] = *(const u32x2*)(P.b1 + f0 + 8 * G);
    STAMP_AT(1);
    kgen_unit(kacc, kpar, lane, pA, pB, pC, dtv);
    if (has1) static_for<EARLY, RING>([&](auto ic) NOPK __attribute__((always_inline)) { constexpr int i = ic.value; issue_w(W1, i, ring + i * 2048); });
    else static_for<EARLY, RING>([&](auto ic) NOPK __attribute__((always_inline)) { constexpr int i = ic.value; issue_w(W2, i, ring + i * 2048); });
    kgen_unit(kacc, kpar, lane, __shfl_down_16(pA), __shfl_down_16(pB), __shfl_down_16(pC), dtv);
    wait_vm0();
    if (has1) preload_regs(WR, W1, ring, ra0, ra1); else preload_regs(WR, W2, ring, ra0, ra1);
    kgen_unit(kacc, kpar, lane, __shfl_down_32(pA), __shfl_down_32(pB), __shfl_down_32(pC), dtv);
    kgen_store(kacc, kpp, lane);
    wait_lgkm0();
    if (lane == 0) lds_add1(ctl + CW_KG * 4);
    STAMP_AT(2);
    if (w == 0) {
      poll_flags((const u64*)(ws + WS_F1) + xcd * 256, 256, tag, lane);
      ACQ();
      if (lane == 0) lds_st(ctl + CW_F1 * 4, 1);
      if (bid == 0 && lane == 0) __hip_atomic_store((GAS unsigned*)(ws + WS_GEN), (unsigned)(tag >> 32), __ATOMIC_RELAXED, __HIP_MEMORY_SCOPE_AGENT);
    } else {
      lds_wait_ge<2>(ctl + CW_F1 * 4, 1);
      wait_vm0();
    }
    STAMP_AT(3);
    if (has1) {
      f32x16 acc[2][2] = {};
      const char* abase = uni_ptr(ws + WS_HF + ((size_t)dt1 * 128 + w) * 2048);
      __builtin_amdgcn_s_setprio(2);
      gemm_loop<32>(acc, WR, W1, abase, lane * 16, ring, ra0, ra1 LST_PASS);
      __builtin_amdgcn_s_setprio(0);
      STAMP_AT(4);
      f32x16 r;
      gbt += 4;
      reduce_quadrants(acc, r, smem, 6 * 2048, w, lane, ctl + CW_GB * 4, gbt);
      unsigned pk[4][2];
#pragma unroll
      for (int G = 0; G < 4; G++) {
        float bv[4] = {bflo(b1v[G][0]), bfhi(b1v[G][0]), bflo(b1v[G][1]), bfhi(b1v[G][1])};
        float o[4];
#pragma unroll
        for (int j = 0; j < 4; j++) {
          float u = rbf(rbf(r[4 * G + j]) + bv[j]);
          float pq = __builtin_fmaf(u * u, 0.1029432397f, 2.302208198f);
          float t = __builtin_amdgcn_exp2f(u * pq);
          o[j] = __builtin_fmaf(-u, __builtin_amdgcn_rcpf(t + 1.f), u);
        }
        pk[G][0] = pk_bf16(o[0], o[1]); pk[G][1] = pk_bf16(o[2], o[3]);
      }
      __amdgpu_buffer_rsrc_t ur = mk_rsrc(ws + WS_UF, 2u << 20);
#pragma unroll
      for (int kp = 0; kp < 2; kp++) {
        auto s0_ = __builtin_amdgcn_permlane32_swap(pk[2 * kp][0], pk[2 * kp + 1][0], false, false);
        auto s1_ = __builtin_amdgcn_permlane32_swap(pk[2 * kp][1], pk[2 * kp + 1][1], false, false);
        u32x4 v = {s0_[0], s1_[0], s0_[1], s1_[1]};
        int kk = ft * 4 + mi * 2 + kp;
        st16_sc1(ur, (((unsigned)dt1 * 112 + kk) * 2 + ni) * 1024 + lane * 16, v);
      }
      static_for<0, 6>([&](auto ic) NOPK __attribute__((always_inline)) { constexpr int i = ic.value; issue_w(W2, i, ring + i * 2048); });
      asm volatile("s_waitcnt vmcnt(12)" ::: "memory");
      gbt += 4;
      group_barrier(ctl + CW_GB * 4, gbt, lane);
      if (w == 0) post_flag(ws, WS_F2, dt1 * 32 + ft, tag, lane);
      static_for<6, RING>([&](auto ic) NOPK __attribute__((always_inline)) { constexpr int i = ic.value; issue_w(W2, i, ring + i * 2048); });
      STAMP_AT(5);
      wait_vm0();
      preload_regs(WR, W2, ring, ra0, ra1);
    }
    const int sq0 = st * 64 + mi * 32 + 4 * h;
    u32x2 b2v[4];
#pragma unroll
    for (int G = 0; G < 4; G++) b2v[G] = *(const u32x2*)(P.b2 + sq0 + 8 * G);
    if (w == 0) {
      poll_flags((const u64*)(ws + WS_F2) + xcd * 256 + dt2 * 32, 28, tag, lane);
      ACQ();
      if (lane == 0) lds_st(ctl + CW_F2 * 4, 1);
    } else {
      lds_wait_ge<2>(ctl + CW_F2 * 4, 1);
      wait_vm0();
    }
    STAMP_AT(6);
    {
      f32x16 acc[2][2] = {};
      const char* abase = uni_ptr(ws + WS_UF + ((size_t)dt2 * 112 + w) * 2048);
      __builtin_amdgcn_s_setprio(2);
      gemm_loop<28>(acc, WR, W2, abase, lane * 16, ring, ra0, ra1 LST_PASS);
      __builtin_amdgcn_s_setprio(0);
      STAMP_AT(7);
      poll_flags((const u64*)(ws + WS_F3) + xcd * 256 + dt2 * 32, 32, tag, lane);
      const int d = dt2 * 64 + ni * 32 + (lane & 31);
      __amdgpu_buffer_rsrc_t ar = mk_rsrc(ws + WS_AT, 2u << 20);
      __amdgpu_buffer_rsrc_t gr = mk_rsrc(ws + WS_G, 8192);
      u32x2 av[4]; u32x4 gvv[4];
#pragma unroll
      for (int G = 0; G < 4; G++) {
        av[G] = ld8_sc1(ar, ((unsigned)d * L + sq0 + 8 * G) * 2);
        gvv[G] = ld16_sc1(gr, (sq0 + 8 * G) * 4);
      }
      f32x16 r;
      gbt += 4;
      reduce_quadrants(acc, r, smem, 0, w, lane, ctl + CW_GB * 4, gbt);
      u16* tl = (u16*)(smem + w * (RING * 2048) + 12288);
#pragma unroll
      for (int G = 0; G < 4; G++) {
        float bv[4] = {bflo(b2v[G][0]), bfhi(b2v[G][0]), bflo(b2v[G][1]), bfhi(b2v[G][1])};
        float aa[4] = {bflo(av[G][0]), bfhi(av[G][0]), bflo(av[G][1]), bfhi(av[G][1])};
#pragma unroll
        for (int j = 0; j < 4; j++) {
          float g = u2f(gvv[G][j]);
          float b = rbf(rbf(r[4 * G + j]) + bv[j]);
          float o = rbf(g * aa[j]) + rbf(rbf(1.f - g) * b);
          tl[(8 * G + 4 * h + j) * 40 + (lane & 31)] = (u16)(pk_bf16(o, 0.f) & 0xffff);
        }
      }
      wait_lgkm0();
      __builtin_amdgcn_wave_barrier();
#pragma unroll
      for (int it = 0; it < 2; it++) {
        int rr = it * 16 + (lane >> 2), cc = (lane & 3) * 8;
        u32x4 v = *(const u32x4*)(tl + rr * 40 + cc);
        *(u32x4*)(P.out + (size_t)(st * 64 + mi * 32 + rr) * D + dt2 * 64 + ni * 32 + cc) = v;
      }
    }
    STAMP_OUT(0, 8);
  } else {
    const int aw = w - 4;
    unsigned abt = 4;
    const int chl = aw >> 1, hw = aw & 1;
    const int d = cpair * 2 + chl;
    char* cs = smem + LDS_AUX + chl * C_SIZE;
    const unsigned pb = ctl + (CW_P0 + chl) * 4;
    unsigned pbt = 0;
    const int r = lane & 15, g = lane >> 4;
    const float dsk = bflo(P.Dskip[d]);
    lds_wait_ge(ctl + CW_KG * 4, 8);
    int sk_;
    {
      const float* kp = (const float*)(cs + C_KC);
      float kv[11];
      unsigned kmax = 0;
#pragma unroll
      for (int i = 0; i < 11; i++) {
        int idx = i * 64 + lane;
        kv[i] = idx < 641 ? (kp[idx] + kp[656 + idx]) + (kp[1312 + idx] + kp[1968 + idx]) : 0.f;
        kmax = max(kmax, f2u(kv[i]) & 0x7fffffffu);
      }
      kmax = wave_umax(kmax);
      int ek = (int)(kmax >> 23) - 127;
      sk_ = kmax ? 14 - ek : 0;
      u16* kh = (u16*)(cs + C_KH);
#pragma unroll
      for (int i = 0; i < 11; i++) {
        int idx = i * 64 + lane;
        if ((i & 1) == hw || i == 10) {
          _Float16 hv = (_Float16)__builtin_amdgcn_ldexpf(kv[i], sk_);
          if (idx < 641) kh[idx] = __builtin_bit_cast(u16, hv);
        }
      }
    }
    pbt += 2;
    group_barrier(pb, pbt, lane);
    {
      const u16* kh = (const u16*)(cs + C_KH);
      if (lane < 8 && hw == 0) {
        *(u32x4*)(cs + C_KC + (lane >> 1) * 4160 + (lane & 1) * 16) = u32x4{0, 0, 0, 0};
      }
#pragma unroll
      for (int it = 0; it < 2; it++) {
        int c = hw * 128 + it * 64 + lane;
        int X = 8 * c;
        unsigned kvv[11];
#pragma unroll
        for (int j = 0; j < 11; j++) {
          int l = X - 3 + j;
          kvv[j] = kh[compact_idx(max(l, 0))];
        }
        if (c == 0) { kvv[0] = 0; kvv[1] = 0; kvv[2] = 0; }
#pragma unroll
        for (int q = 0; q < 4; q++) {
          u32x4 o;
#pragma unroll
          for (int e = 0; e < 4; e++) o[e] = kvv[3 - q + 2 * e] | (kvv[3 - q + 2 * e + 1] << 16);
          *(u32x4*)(cs + C_KC + q * 4160 + 32 + c * 16) = o;
        }
      }
    }
    STAMP_AT(3);
    lds_wait_ge<2>(ctl + CW_F1 * 4, 1);
    wait_vm0();
    STAMP_AT(4);
    int su;
    {
      __amdgpu_buffer_rsrc_t xr_ = mk_rsrc(ws + WS_XC, 2u << 20);
      u32x4 xr[2];
#pragma unroll
      for (int i = 0; i < 2; i++) xr[i] = ld16_x(xr_, ((unsigned)(hw * 128 + i * 64 + lane) * 512 + d) * 16);
      unsigned umax = 0;
#pragma unroll
      for (int i = 0; i < 2; i++)
#pragma unroll
        for (int j = 0; j < 4; j++) { umax = max(umax, (xr[i][j] & 0x7fff)); umax = max(umax, (xr[i][j] >> 16) & 0x7fff); }
      umax = wave_umax(umax);
      unsigned* red = (unsigned*)(cs + C_PAR + 1024);
      if (lane == 0) red[hw] = umax;
      pbt += 2;
      group_barrier(pb, pbt, lane);
      umax = max(red[0], red[1]);
      int eu = (int)(umax >> 7) - 127;
      su = umax ? 14 - eu : 0;
      u16* u16p = (u16*)(cs + C_U16);
      u16* skp = (u16*)(cs + C_SK);
      if (hw == 0 && lane < 32) *(u32x4*)(u16p + lane * 8) = u32x4{0, 0, 0, 0};
#pragma unroll
      for (int i = 0; i < 2; i++) {
        int sb = hw * 128 + i * 64 + lane;
        u32x4 o, sk;
#pragma unroll
        for (int j = 0; j < 4; j++) {
          float a = bflo(xr[i][j]), b = bfhi(xr[i][j]);
          o[j] = __builtin_bit_cast(unsigned, __builtin_amdgcn_cvt_pkrtz(__builtin_amdgcn_ldexpf(a, su), __builtin_amdgcn_ldexpf(b, su)));
          sk[j] = pk_bf16(a * dsk, b * dsk);
        }
        *(u32x4*)(u16p + 256 + sb * 8) = o;
        *(u32x4*)(skp + sb * 8) = sk;
      }
    }
    pbt += 2;
    group_barrier(pb, pbt, lane);
    f32x4 yacc[8] = {};
    {
      const int qq = (lane & 15) >> 2, pp = lane & 3;
      const unsigned cb = (unsigned)(size_t)(LAS char*)cs;
      const unsigned aaddr = cb + C_KC + qq * 4160 + (16 - 8 * g + 4 * pp + 16) * 2;
      const unsigned baddr = cb + C_U16 + (256 - 16 + 16 * r + 8 * g) * 2;
#pragma unroll
      for (int rr = 0; rr < 4; rr++) {
        const int rho = hw * 4 + rr;
        f16x8 af[8];
#pragma unroll
        for (int al = 0; al < 8; al++) {
          unsigned ad = aaddr + (rho + 8 * al) * 64;
          s16x4 a0 = tr_read(ad), a1 = tr_read(ad - 8);
          af[al] = __builtin_bit_cast(f16x8, __builtin_shufflevector(a0, a1, 0, 1, 2, 3, 4, 5, 6, 7));
        }
#pragma unroll
        for (int mu = 0; mu < 8; mu++) {
          f16x8 bf = *(const LAS f16x8*)(size_t)(baddr + (8 * mu - rho) * 64);
#pragma unroll
          for (int al = 0; al + mu < 8; al++)
            yacc[mu + al] = __builtin_amdgcn_mfma_f32_16x16x32_f16(af[al], bf, yacc[mu + al], 0, 0, 0);
        }
      }
    }
    STAMP_AT(5);
    {
      pbt += 2;
      group_barrier(pb, pbt, lane);
      f32x4* ex = (f32x4*)(cs + C_KC);
      f32x4 mine[4];
#pragma unroll
      for (int i = 0; i < 4; i++) {
        f32x4 snd;
#pragma unroll
        for (int e = 0; e < 4; e++) {
          float a = yacc[i][e], b = yacc[4 + i][e];
          asm volatile("" : "+v"(a), "+v"(b));
          snd[e] = hw ? a : b; mine[i][e] = hw ? b : a;
        }
        ex[(hw * 4 + i) * 64 + lane] = snd;
      }
      pbt += 2;
      group_barrier(pb, pbt, lane);
      __amdgpu_buffer_rsrc_t ar_ = mk_rsrc(ws + WS_AT, 2u << 20);
      const int sc = -(su + sk_);
#pragma unroll
      for (int i = 0; i < 4; i++) {
        int ti = hw * 4 + i;
        f32x4 o = ex[((1 - hw) * 4 + i) * 64 + lane];
        f32x4 me = mine[i];
        int tt = 256 * ti + 16 * r + 4 * g;
        float y0 = rbf(__builtin_amdgcn_ldexpf(me[0] + o[0], sc)), y1 = rbf(__builtin_amdgcn_ldexpf(me[1] + o[1], sc));
        float y2 = rbf(__builtin_amdgcn_ldexpf(me[2] + o[2], sc)), y3 = rbf(__builtin_amdgcn_ldexpf(me[3] + o[3], sc));
        u32x2 sk = *(const u32x2*)((const u16*)(cs + C_SK) + tt);
        u32x2 res = {pk_bf16(y0 + bflo(sk[0]), y1 + bfhi(sk[0])), pk_bf16(y2 + bflo(sk[1]), y3 + bfhi(sk[1]))};
        st8_sc1(ar_, ((unsigned)d * L + tt) * 2, res);
      }
      wait_vm0();
      abt += 4;
      group_barrier(ctl + CW_AB * 4, abt, lane);
      if (aw == 0) post_flag(ws, WS_F3, cpair, tag, lane);
    }
    STAMP_AT(6);
    STAMP_OUT(1, 7);
  }
}

extern "C" void kernel_launch(void* const* d_in, const int* in_sizes, int n_in,
                              void* d_out, int out_size, void* d_ws, size_t ws_size,
                              hipStream_t stream) {
  static bool attr = false;
  if (!attr) {
    (void)hipFuncSetAttribute((const void*)mega_kernel, hipFuncAttributeMaxDynamicSharedMemorySize, LDS_TOTAL);
    attr = true;
  }
  Params P;
  P.x = (const u16*)d_in[0]; P.Are = (const u16*)d_in[1]; P.Aim = (const u16*)d_in[2]; P.Bre = (const u16*)d_in[3]; P.Bim = (const u16*)d_in[4];
  P.Cre = (const u16*)d_in[5]; P.Cim = (const u16*)d_in[6]; P.logdt = (const u16*)d_in[7]; P.Dskip = (const u16*)d_in[8];
  P.b1 = (const u16*)d_in[11]; P.b2 = (const u16*)d_in[13]; P.gw = (const u16*)d_in[14];
  P.w1 = (const char*)d_in[10]; P.w2 = (const char*)d_in[12];
  P.ws = (char*)d_ws; P.out = (u16*)d_out;
  mega_kernel<<<256, 512, LDS_TOTAL, stream>>>(P);
}
```

```cpp
#include <hip/hip_runtime.h>
#include <stdint.h>
#include <type_traits>

typedef unsigned short u16;
typedef unsigned long long u64;
typedef short s16x4 __attribute__((ext_vector_type(4)));
typedef __bf16 bf16x8 __attribute__((ext_vector_type(8)));
typedef _Float16 f16x8 __attribute__((ext_vector_type(8)));
typedef float f32x2 __attribute__((ext_vector_type(2)));
typedef float f32x4 __attribute__((ext_vector_type(4)));
typedef float f32x16 __attribute__((ext_vector_type(16)));
typedef unsigned u32x2 __attribute__((ext_vector_type(2)));
typedef unsigned u32x4 __attribute__((ext_vector_type(4)));
#define LAS __attribute__((address_space(3)))
#define GAS __attribute__((address_space(1)))
#if defined(__HIP_DEVICE_COMPILE__)
#define NOPK __attribute__((target("no-packed-fp32-ops")))
#else
#define NOPK
#endif
#define DEVI __device__ __forceinline__ NOPK

constexpr int L = 2048, D = 512, NM = 128, F = 1792;

DEVI float u2f(unsigned v) { return __builtin_bit_cast(float, v); }
DEVI unsigned f2u(float v) { return __builtin_bit_cast(unsigned, v); }
DEVI float bflo(unsigned v) { return u2f(v << 16); }
DEVI float bfhi(unsigned v) { return u2f(v & 0xffff0000u); }
DEVI unsigned pk_bf16(float lo, float hi) {
  typedef __bf16 b2 __attribute__((ext_vector_type(2)));
  f32x2 v = {lo, hi};
  b2 r = __builtin_convertvector(v, b2);
  return __builtin_bit_cast(unsigned, r);
}
DEVI float fmaxf_(float a, float b) { return __builtin_fmaxf(a, b); }
DEVI float rbf(float f) { return bflo(pk_bf16(f, 0.f)); }

constexpr size_t WS_HF = 0;
constexpr size_t WS_XC = 2u << 20;
constexpr size_t WS_UF = 4u << 20;
constexpr size_t WS_AT = 6u << 20;
constexpr size_t WS_G  = 8u << 20;
constexpr size_t WS_F1 = (8u << 20) + 65536;
constexpr size_t WS_F2 = WS_F1 + 16384;
constexpr size_t WS_F3 = WS_F2 + 16384;
constexpr size_t WS_GEN = WS_F3 + 16384;

constexpr int RING = 12, PACT = 8, NWR = 10;
constexpr int GU = 1;
constexpr int WARM0 = 1;
constexpr int LDS_RING = 0;
constexpr int LDS_AUX = 4 * RING * 2048;
constexpr int C_PAR = 0, C_SK = 2048, C_KH = 7296, C_KC = 8640, C_U16 = 25280, C_SIZE = 29888;
constexpr int LDS_CTL = LDS_AUX + 2 * C_SIZE;
constexpr int LDS_LNST = LDS_CTL + 256;
constexpr int LDS_TOTAL = 163840;
enum { CW_TAGV = 0, CW_TAG = 1, CW_F1 = 2, CW_GB = 3, CW_AB = 4, CW_P0 = 5, CW_P1 = 6, CW_KG = 7, CW_F2 = 8, CW_LS = 9 };

template <int I, int N, class Fn>
DEVI void static_for(Fn&& f) {
  if constexpr (I < N) { f(std::integral_constant<int, I>{}); static_for<I + 1, N>(f); }
}
DEVI void dma16(unsigned lds_addr, const void* sbase, unsigned voff) {
  unsigned keep;
  asm volatile("s_mov_b32 %0, m0\n\ts_mov_b32 m0, %1\n\ts_nop 0\n\tglobal_load_lds_dwordx4 %2, %3\n\ts_mov_b32 m0, %0"
               : "=&s"(keep) : "s"(lds_addr), "v"(voff), "s"(sbase) : "memory");
}
template <int OFF>
DEVI void gload16_sc1(u32x4& dst, const void* sbase, unsigned voff) {
  asm volatile("global_load_dwordx4 %0, %1, %2 offset:%3 sc1" : "=v"(dst) : "v"(voff), "s"(sbase), "i"(OFF) : "memory");
}
template <int N>
DEVI void wait_vm2(u32x4& a, u32x4& b) {
  asm volatile("s_waitcnt vmcnt(%2)" : "+v"(a), "+v"(b) : "i"(N) : "memory");
}
DEVI const char* uni_ptr(const char* p) {
  u64 v = (u64)p;
  unsigned lo = __builtin_amdgcn_readfirstlane((unsigned)v), hi = __builtin_amdgcn_readfirstlane((unsigned)(v >> 32));
  return (const char*)(((u64)hi << 32) | lo);
}
DEVI void wait_vm0() { asm volatile("s_waitcnt vmcnt(0)" ::: "memory"); }
DEVI void wait_lgkm0() { asm volatile("s_waitcnt lgkmcnt(0)" ::: "memory"); }
DEVI s16x4 tr_read(unsigned addr) {
  return __builtin_amdgcn_ds_read_tr16_b64_v4i16((LAS s16x4*)(size_t)addr);
}
DEVI __amdgpu_buffer_rsrc_t mk_rsrc(const void* p, unsigned bytes) {
  return __builtin_amdgcn_make_buffer_rsrc((void*)p, 0, bytes, 0x00020000);
}
DEVI u32x4 ld16_sc1(__amdgpu_buffer_rsrc_t r, unsigned off) { return __builtin_amdgcn_raw_buffer_load_b128(r, off, 0, 16); }
DEVI u32x4 ld16_x(__amdgpu_buffer_rsrc_t r, unsigned off) { return __builtin_amdgcn_raw_buffer_load_b128(r, off, 0, 16); }
DEVI u32x2 ld8_sc1(__amdgpu_buffer_rsrc_t r, unsigned off) { return __builtin_amdgcn_raw_buffer_load_b64(r, off, 0, 16); }
DEVI void st16_sc1(__amdgpu_buffer_rsrc_t r, unsigned off, u32x4 v) { __builtin_amdgcn_raw_buffer_store_b128(v, r, off, 0, 16); }
DEVI void st8_sc1(__amdgpu_buffer_rsrc_t r, unsigned off, u32x2 v) { __builtin_amdgcn_raw_buffer_store_b64(v, r, off, 0, 16); }
DEVI void st4_sc1(__amdgpu_buffer_rsrc_t r, unsigned off, unsigned v) { __builtin_amdgcn_raw_buffer_store_b32(v, r, off, 0, 16); }

template <int CTRL, int RM>
DEVI float dpp_f(float v) { return u2f(__builtin_amdgcn_update_dpp(0, f2u(v), CTRL, RM, 0xf, true)); }
template <int CTRL, int RM>
DEVI unsigned dpp_u(unsigned v) { return __builtin_amdgcn_update_dpp(0, v, CTRL, RM, 0xf, true); }
DEVI float wave_sum(float v) {
  v += dpp_f<0x111, 0xf>(v); v += dpp_f<0x112, 0xf>(v); v += dpp_f<0x114, 0xf>(v); v += dpp_f<0x118, 0xf>(v);
  v += dpp_f<0x142, 0xa>(v); v += dpp_f<0x143, 0xc>(v);
  return u2f(__builtin_amdgcn_readlane(f2u(v), 63));
}
DEVI unsigned wave_umax(unsigned v) {
  v = max(v, dpp_u<0x111, 0xf>(v)); v = max(v, dpp_u<0x112, 0xf>(v)); v = max(v, dpp_u<0x114, 0xf>(v)); v = max(v, dpp_u<0x118, 0xf>(v));
  v = max(v, dpp_u<0x142, 0xa>(v)); v = max(v, dpp_u<0x143, 0xc>(v));
  return __builtin_amdgcn_readlane(v, 63);
}

DEVI unsigned lds_ld(unsigned addr) {
  unsigned v;
  asm volatile("ds_read_b32 %0, %1\n\ts_waitcnt lgkmcnt(0)" : "=v"(v) : "v"(addr) : "memory");
  return __builtin_amdgcn_readfirstlane(v);
}
DEVI void lds_st(unsigned addr, unsigned v) { asm volatile("ds_write_b32 %0, %1" :: "v"(addr), "v"(v) : "memory"); }
DEVI void lds_add1(unsigned addr) { asm volatile("ds_add_u32 %0, %1" :: "v"(addr), "v"(1u) : "memory"); }
constexpr unsigned SPIN_CAP = 1u << 22;
template <int SLP = 1>
DEVI void lds_wait_ge(unsigned addr, unsigned target) {
  for (unsigned sp = 0; sp < SPIN_CAP; sp++) {
    if (lds_ld(addr) >= target) break;
    __builtin_amdgcn_s_sleep(SLP);
  }
}
DEVI void group_barrier(unsigned addr, unsigned target, int lane) {
  wait_lgkm0();
  if (lane == 0) lds_add1(addr);
  lds_wait_ge(addr, target);
}

DEVI void poll_flags(const u64* flags, int n, u64 tag, int lane) {
  const GAS u64* f = (const GAS u64*)flags;
  for (unsigned sp = 0; sp < (1u << 20); sp++) {
    bool ok = true;
#pragma unroll
    for (int j = 0; j < 4; j++) {
      int i = j * 64 + lane;
      if (j * 64 < n) {
        u64 v = (i < n) ? __hip_atomic_load(f + i, __ATOMIC_RELAXED, __HIP_MEMORY_SCOPE_AGENT) : tag;
        ok &= (v == tag);
      }
    }
    if ((__builtin_amdgcn_ballot_w64(ok) == ~0ull)) break;
    __builtin_amdgcn_s_sleep(2);
  }
}

struct WStream { const char* base; unsigned step, v0, v1; int rot, ns; };
DEVI unsigned rot_step(int step, int rot, int ns) { int k = step + rot; return (unsigned)(k >= ns ? k - ns : k); }
DEVI void issue_w(const WStream& ws_, int step, unsigned slot) {
  const char* b = ws_.base + (size_t)(rot_step(step, ws_.rot, ws_.ns) * ws_.step);
  dma16(slot, b, ws_.v0);
  dma16(slot + 1024, b, ws_.v1);
}
template <int NS> constexpr int pos2step(int i) { return i < RING ? NWR + i : (i < RING + NWR ? i - RING : i); }
template <int NS> constexpr int n_rem() { return NS - NWR - RING; }
template <int NS> constexpr int gemm_ops_at(int p) { return (p < n_rem<NS>() ? 2 : 0) + ((p + PACT < NS) ? 2 : 0); }
template <int NS> constexpr int gemm_cnt(int i) {
  int cnt = 0;
  int last = i - 2;
  if (i < PACT) { cnt = 2 * (PACT - 1 - i); for (int s = 0; s <= last; s++) cnt += gemm_ops_at<NS>(s); }
  else { for (int s = i - PACT + 1; s <= last; s++) cnt += gemm_ops_at<NS>(s); }
  return cnt;
}
template <int NS> constexpr int gemm_cnt2(int i) {
  int cnt = 0;
  int first = 0;
  if (i < PACT) { cnt = 2 * (PACT - 1 - i); first = 0; }
  else first = i - PACT + 1;
  for (int s = first; s <= i - 2; s++) cnt += gemm_ops_at<NS>(s);
  if (i - 1 >= first && i - 1 < n_rem<NS>()) cnt += 2;
  return cnt;
}
typedef s16x4 WFrag[4];
template <int OFF>
DEVI void tr_asm(s16x4& d, unsigned addr) {
  asm volatile("ds_read_b64_tr_b16 %0, %1 offset:%2" : "=v"(d) : "v"(addr), "i"(OFF) : "memory");
}
template <int SLOT>
DEVI void read_w(WFrag& n, unsigned a0, unsigned a1) {
  tr_asm<SLOT * 2048>(n[0], a0); tr_asm<SLOT * 2048 + 512>(n[1], a0);
  tr_asm<SLOT * 2048>(n[2], a1); tr_asm<SLOT * 2048 + 512>(n[3], a1);
}
template <int N>
DEVI void wait_lgkm_w(WFrag& f) {
  asm volatile("s_waitcnt lgkmcnt(%4)" : "+v"(f[0]), "+v"(f[1]), "+v"(f[2]), "+v"(f[3]) : "i"(N) : "memory");
}
DEVI void preload_regs(WFrag (&WR)[NWR], const WStream& wc, unsigned ring, unsigned a0, unsigned a1) {
  static_for<0, NWR>([&](auto ic) NOPK __attribute__((always_inline)) {
    constexpr int j = ic.value;
    read_w<j>(WR[j], a0, a1);
    if constexpr ((j % 3) == 2 || j == NWR - 1) {
      wait_lgkm_w<0>(WR[j]);
      if constexpr (j % 3 >= 1) wait_lgkm_w<0>(WR[j - 1]);
      if constexpr (j % 3 >= 2) wait_lgkm_w<0>(WR[j - 2]);
    }
  });
  static_for<0, NWR>([&](auto ic) NOPK __attribute__((always_inline)) { constexpr int j = ic.value; issue_w(wc, RING + j, ring + j * 2048); });
}
template <int NS>
DEVI void gemm_loop(f32x16 (&acc)[2][2], WFrag (&WR)[NWR], const WStream& wc, const char* abase, unsigned lane16, unsigned ring, unsigned a0_, unsigned a1_) {
  u32x4 act[PACT][2];
  static_for<0, PACT>([&](auto ic) NOPK __attribute__((always_inline)) {
    constexpr int i = ic.value;
    constexpr int sp = pos2step<NS>(i);
    const char* ab = abase + (size_t)(rot_step(sp, wc.rot, NS) * 8192u);
    gload16_sc1<0>(act[i][0], ab, lane16);
    gload16_sc1<1024>(act[i][1], ab, lane16);
  });
  WFrag wt[2];
  wait_vm2<gemm_cnt<NS>(0)>(act[0][0], act[0][1]);
  { constexpr int sp0 = pos2step<NS>(0); read_w<sp0 % RING>(wt[0], a0_, a1_); }
#define SB() __builtin_amdgcn_sched_barrier(0)
  static_for<0, NS>([&](auto ic) NOPK __attribute__((always_inline)) {
    constexpr int i = ic.value;
    constexpr int st = pos2step<NS>(i);
    constexpr bool in_reg = st < NWR;
    constexpr int sn = pos2step<NS>(i + 1 < NS ? i + 1 : 0);
    constexpr bool next_lds = (i + 1 < NS) && (sn >= NWR);
    constexpr int nrem = n_rem<NS>();
    u32x4& a0 = act[i % PACT][0];
    u32x4& a1 = act[i % PACT][1];
    WFrag& c = in_reg ? WR[in_reg ? st : 0] : wt[i & 1];
    if constexpr (!in_reg) wait_lgkm_w<0>(c);
    bf16x8 wf0 = __builtin_bit_cast(bf16x8, __builtin_shufflevector(c[0], c[1], 0, 1, 2, 3, 4, 5, 6, 7));
    bf16x8 wf1 = __builtin_bit_cast(bf16x8, __builtin_shufflevector(c[2], c[3], 0, 1, 2, 3, 4, 5, 6, 7));
    bf16x8 h0 = __builtin_bit_cast(bf16x8, a0), h1 = __builtin_bit_cast(bf16x8, a1);
    SB();
    acc[0][0] = __builtin_amdgcn_mfma_f32_32x32x16_bf16(wf0, h0, acc[0][0], 0, 0, 0);
    SB();
    if constexpr (i < nrem) issue_w(wc, NWR + RING + i, ring + (st % RING) * 2048);
    SB();
    acc[1][0] = __builtin_amdgcn_mfma_f32_32x32x16_bf16(wf1, h0, acc[1][0], 0, 0, 0);
    SB();
    if constexpr (i + 1 < NS) {
      wait_vm2<gemm_cnt2<NS>(i + 1)>(act[(i + 1) % PACT][0], act[(i + 1) % PACT][1]);
      if constexpr (next_lds) read_w<sn % RING>(wt[(i + 1) & 1], a0_, a1_);
    }
    SB();
    acc[0][1] = __builtin_amdgcn_mfma_f32_32x32x16_bf16(wf0, h1, acc[0][1], 0, 0, 0);
    SB();
    constexpr int sp = pos2step<NS>(i + PACT < NS ? i + PACT : 0);
    const char* ab = abase + (size_t)(rot_step(sp, wc.rot, NS) * 8192u);
    if constexpr (i + PACT < NS) gload16_sc1<0>(a0, ab, lane16);
    SB();
    acc[1][1] = __builtin_amdgcn_mfma_f32_32x32x16_bf16(wf1, h1, acc[1][1], 0, 0, 0);
    SB();
    if constexpr (i + PACT < NS) gload16_sc1<1024>(a1, ab, lane16);
    SB();
  });
}

DEVI void reduce_write(f32x16 (&acc)[2][2], char* smem, int xoff, int w, int lane) {
#pragma unroll
  for (int q = 0; q < 4; q++) {
    if (q != w) {
      int sl = ((q - w) & 3) - 1;
      f32x4* p = (f32x4*)(smem + w * (RING * 2048) + xoff + sl * 4096 + lane * 16);
      const f32x16& a = acc[q >> 1][q & 1];
#pragma unroll
      for (int g = 0; g < 4; g++) p[g * 64] = f32x4{a[4 * g], a[4 * g + 1], a[4 * g + 2], a[4 * g + 3]};
    }
  }
}
DEVI void reduce_read(f32x16 (&acc)[2][2], f32x16& res, char* smem, int xoff, int w, int lane, unsigned gb_addr, unsigned gb_target) {
  group_barrier(gb_addr, gb_target, lane);
  res = (w == 0) ? acc[0][0] : (w == 1) ? acc[0][1] : (w == 2) ? acc[1][0] : acc[1][1];
#pragma unroll
  for (int j = 1; j < 4; j++) {
    int src = (w - j) & 3;
    const f32x4* p = (const f32x4*)(smem + src * (RING * 2048) + xoff + (j - 1) * 4096 + lane * 16);
#pragma unroll
    for (int g = 0; g < 4; g++) {
      f32x4 v = p[g * 64];
      res[4 * g] += v[0]; res[4 * g + 1] += v[1]; res[4 * g + 2] += v[2]; res[4 * g + 3] += v[3];
    }
  }
}
DEVI void reduce_quadrants(f32x16 (&acc)[2][2], f32x16& res, char* smem, int xoff, int w, int lane, unsigned gb_addr, unsigned gb_target) {
  reduce_write(acc, smem, xoff, w, lane);
  reduce_read(acc, res, smem, xoff, w, lane, gb_addr, gb_target);
}

DEVI void cexp_tab(float cr, float ci, float l, float& er, float& ei) {
  float mag = __builtin_amdgcn_exp2f(cr * l);
  float rev = __builtin_amdgcn_fractf(ci * l);
  er = mag * __builtin_amdgcn_cosf(rev);
  ei = mag * __builtin_amdgcn_sinf(rev);
}
DEVI void split8(const float (&v)[8], bf16x8& hi, bf16x8& lo) {
  u32x4 h, l;
#pragma unroll
  for (int i = 0; i < 4; i++) {
    unsigned p = pk_bf16(v[2 * i], v[2 * i + 1]);
    h[i] = p;
    l[i] = pk_bf16(v[2 * i] - bflo(p), v[2 * i + 1] - bfhi(p));
  }
  hi = __builtin_bit_cast(bf16x8, h); lo = __builtin_bit_cast(bf16x8, l);
}
DEVI int compact_idx(int l) {
  float f = (float)l;
  unsigned u = f2u(f);
  u = (u + 0x7FFF + ((u >> 16) & 1)) >> 16;
  return l < 128 ? l : (int)(u - 0x4300) + 128;
}

struct Params {
  const u16 *x, *Are, *Aim, *Bre, *Bim, *Cre, *Cim, *logdt, *Dskip, *b1, *b2, *gw;
  const char *w1, *w2;
  char* ws;
  u16* out;
};

DEVI void post_flag(char* ws, size_t base, int idx, u64 tag, int lane) {
  if (lane < 8) __hip_atomic_store((GAS u64*)(ws + base) + lane * 256 + idx, tag, __ATOMIC_RELAXED, __HIP_MEMORY_SCOPE_AGENT);
}
DEVI void wg_barrier() { wait_lgkm0(); __builtin_amdgcn_s_barrier(); }
DEVI void gl16(u32x4& dst, const void* sbase, unsigned voff) {
  asm volatile("global_load_dwordx4 %0, %1, %2" : "=v"(dst) : "v"(voff), "s"(sbase) : "memory");
}
template <int OFF>
DEVI void glu16(unsigned& dst, const void* sbase, unsigned voff) {
  asm volatile("global_load_ushort %0, %1, %2 offset:%3" : "=v"(dst) : "v"(voff), "s"(sbase), "i"(OFF) : "memory");
}
template <int OFF>
DEVI void gl4o(unsigned& dst, const void* sbase, unsigned voff) {
  asm volatile("global_load_dword %0, %1, %2 offset:%3" : "=v"(dst) : "v"(voff), "s"(sbase), "i"(OFF) : "memory");
}
DEVI void gl4_sc1(unsigned& dst, const void* sbase, unsigned voff) {
  asm volatile("global_load_dword %0, %1, %2 sc1" : "=v"(dst) : "v"(voff), "s"(sbase) : "memory");
}

DEVI unsigned __shfl_down_16(unsigned v) { return (unsigned)__builtin_amdgcn_ds_bpermute((int)(((threadIdx.x & 63) + 16) & 63) * 4, (int)v); }
DEVI unsigned __shfl_down_32(unsigned v) { return (unsigned)__builtin_amdgcn_ds_bpermute((int)(((threadIdx.x & 63) + 32) & 63) * 4, (int)v); }
DEVI void kgen_unit(f32x4 (&kacc)[3], char* par, int lane, unsigned pA, unsigned pB, unsigned pC, float dt) {
  {
    float ar = dt * bflo(pA), ai = dt * bfhi(pA), br = dt * bflo(pB), bi = dt * bfhi(pB), cr = bflo(pC), ci = bfhi(pC);
    f32x4 pv = {ar * 1.4426950408889634f, ai * 0.15915494309189535f, cr * br - ci * bi, cr * bi + ci * br};
    if (lane < 16) *(f32x4*)(par + lane * 16) = pv;
  }
  wait_lgkm0();
  __builtin_amdgcn_wave_barrier();
  const int r = lane & 15, g = lane >> 4;
  f32x4 pv[4];
#pragma unroll
  for (int i = 0; i < 4; i++) pv[i] = *(const f32x4*)(par + (4 * g + i) * 16);
  float v[8];
  bf16x8 bh, bl;
  {
    const float lo = (float)(8 * r);
#pragma unroll
    for (int i = 0; i < 4; i++) {
      float er, ei;
      cexp_tab(pv[i][0], pv[i][1], lo, er, ei);
      v[2 * i] = pv[i][2] * er - pv[i][3] * ei;
      v[2 * i + 1] = -(pv[i][2] * ei + pv[i][3] * er);
    }
    split8(v, bh, bl);
  }
#pragma unroll
  for (int T = 0; T < 3; T++) {
    const float hv = T == 0 ? (float)((r & 7) + 128 * (r >> 3))
                   : T == 1 ? (r < 8 ? (float)(256 + 2 * (r & 3) + 128 * ((r >> 2) & 1)) : (float)(512 + 4 * (r & 1) + 128 * ((r >> 1) & 3)))
                            : (r < 8 ? (float)(1024 + 128 * r) : 2048.f);
#pragma unroll
    for (int i = 0; i < 4; i++) cexp_tab(pv[i][0], pv[i][1], hv, v[2 * i], v[2 * i + 1]);
    bf16x8 ah, al;
    split8(v, ah, al);
    kacc[T] = __builtin_amdgcn_mfma_f32_16x16x32_bf16(ah, bh, kacc[T], 0, 0, 0);
    kacc[T] = __builtin_amdgcn_mfma_f32_16x16x32_bf16(ah, bl, kacc[T], 0, 0, 0);
    kacc[T] = __builtin_amdgcn_mfma_f32_16x16x32_bf16(al, bh, kacc[T], 0, 0, 0);
  }
  wait_lgkm0();
}
DEVI void kgen_store(const f32x4 (&kacc)[3], float* kp, unsigned* pmax, int lane) {
  const int b = lane & 15, g = lane >> 4;
  {
    unsigned mx = 0;
#pragma unroll
    for (int T = 0; T < 3; T++)
#pragma unroll
      for (int e = 0; e < 4; e++) mx = max(mx, f2u(kacc[T][e]) & 0x7fffffffu);
    mx = wave_umax(mx);
    if (lane == 0) *pmax = mx;
  }
#pragma unroll
  for (int reg = 0; reg < 4; reg++) {
    int row = 4 * g + reg;
    kp[(row & 7) + 128 * (row >> 3) + 8 * b] = kacc[0][reg];
    int i1 = row < 8 ? 256 + (row & 3) + 64 * ((row >> 2) & 1) + 4 * b : 384 + (row & 1) + 32 * ((row >> 1) & 3) + 2 * b;
    kp[i1] = kacc[1][reg];
    if (row < 8) kp[512 + 16 * row + b] = kacc[2][reg];
    else if (row == 8 && b == 0) kp[640] = kacc[2][reg];
  }
}
DEVI unsigned shfl_from(unsigned v, int lane, int off) { return (unsigned)__builtin_amdgcn_ds_bpermute(((lane + off) & 63) * 4, (int)v); }

NOPK __global__ __launch_bounds__(512, 2) void mega_kernel(Params P) {
  extern __shared__ __attribute__((aligned(16))) char smem[];
  char* ws = P.ws;
  const int tid = threadIdx.x;
  const int lane = tid & 63;
  const int w = __builtin_amdgcn_readfirstlane(tid >> 6);
  const int bid = blockIdx.x;
  const unsigned sbase = (unsigned)(size_t)(LAS char*)smem;
  const unsigned ctl = sbase + LDS_CTL;
  const int xcd = bid & 7, xi = bid >> 3;
  const int s0 = bid * 8;
  const bool isg = w < 4;
  const bool has1 = xi < 28;
  const int dt1 = (xcd & 1) * 4 + (xi & 3), ft = (xcd >> 1) * 7 + (has1 ? (xi >> 2) : 0);
  const int dt2 = (xcd & 1) * 4 + (xi & 3), st = (xcd >> 1) * 8 + (xi >> 2);
  const int row = lane >> 3, ch8 = lane & 7;
  const unsigned swz = ((ch8 ^ (((row >> 1) & 1) << 2)) << 4);
  WStream W1, W2;
  W1.base = uni_ptr(P.w1 + (size_t)((w & 3) * 16) * (F * 2) + ft * 128); W1.step = 64 * F * 2; W1.v0 = row * (F * 2) + swz; W1.v1 = W1.v0 + 8 * F * 2;
  W2.base = uni_ptr(P.w2 + (size_t)((w & 3) * 16) * (L * 2) + st * 128); W2.step = 64 * L * 2; W2.v0 = row * (L * 2) + swz; W2.v1 = W2.v0 + 8 * L * 2;
  W1.rot = 0; W2.rot = 0;
  W1.ns = 32; W2.ns = 28;
  const unsigned ring = sbase + LDS_RING + (w & 3) * RING * 2048;
  const int kch = (w & 3) >> 1;
  const int cpair = xcd * 32 + xi;
  const int kd = cpair * 2 + kch;
  u64 tag = 0;
  float* stt = (float*)(smem + LDS_LNST);
  unsigned* red = (unsigned*)(smem + LDS_LNST + 192);
  if (tid < 32) ((unsigned*)(smem + LDS_CTL))[tid] = 0;
  if (!isg) {
    const int a4 = w & 3;
    u32x4 xv[2], gv;
    unsigned col[8], col1[8], gen = 0, pre[7];
    const char* xrow = uni_ptr((const char*)(P.x + (size_t)s0 * D));
    gl16(xv[0], xrow + a4 * 2048, lane * 16);
    gl16(xv[1], xrow + a4 * 2048 + 1024, lane * 16);
    gl16(gv, P.gw, lane * 16);
    const unsigned co = (a4 * 128 + lane) * 2;
    static_for<0, 4>([&](auto ic) NOPK __attribute__((always_inline)) { constexpr int r = ic.value; glu16<r * 1024>(col[r], xrow, co); glu16<r * 1024 + 128>(col1[r], xrow, co); });
    static_for<0, 4>([&](auto ic) NOPK __attribute__((always_inline)) { constexpr int r = ic.value; glu16<r * 1024>(col[4 + r], xrow + 4096, co); glu16<r * 1024 + 128>(col1[4 + r], xrow + 4096, co); });
    gl4_sc1(gen, ws + WS_GEN, 0);
    const unsigned po = ((unsigned)kd * NM + min(32 * GU + 16 * (4 - GU) * (w & 1) + lane, NM - 1)) * 2;
    glu16<0>(pre[0], P.Are, po); glu16<0>(pre[1], P.Aim, po); glu16<0>(pre[2], P.Bre, po); glu16<0>(pre[3], P.Bim, po);
    glu16<0>(pre[4], P.Cre, po); glu16<0>(pre[5], P.Cim, po); glu16<0>(pre[6], P.logdt, kd * 2);
    wg_barrier();
    __builtin_amdgcn_s_setprio(3);
    asm volatile("s_waitcnt vmcnt(0)"
                 : "+v"(xv[0]), "+v"(xv[1]), "+v"(gv), "+v"(col[0]), "+v"(col[1]), "+v"(col[2]), "+v"(col[3]), "+v"(col[4]), "+v"(col[5]), "+v"(col[6]), "+v"(col[7]),
                   "+v"(gen), "+v"(pre[0]), "+v"(pre[1]), "+v"(pre[2]), "+v"(pre[3]), "+v"(pre[4]), "+v"(pre[5]), "+v"(pre[6])
                 :: "memory");
    asm volatile("" : "+v"(col1[0]), "+v"(col1[1]), "+v"(col1[2]), "+v"(col1[3]), "+v"(col1[4]), "+v"(col1[5]), "+v"(col1[6]), "+v"(col1[7]));
    {
      float s1[2] = {0, 0}, s2[2] = {0, 0}, gs[2] = {0, 0}, c0[2];
#pragma unroll
      for (int rr = 0; rr < 2; rr++) {
        c0[rr] = u2f(__builtin_amdgcn_readfirstlane(xv[rr][0]) << 16);
#pragma unroll
        for (int i = 0; i < 4; i++) {
          float a = bflo(xv[rr][i]), b = bfhi(xv[rr][i]);
          gs[rr] += rbf(a * bflo(gv[i])) + rbf(b * bfhi(gv[i]));
          a -= c0[rr]; b -= c0[rr];
          s1[rr] += a + b;
          s2[rr] = __builtin_fmaf(a, a, __builtin_fmaf(b, b, s2[rr]));
        }
      }
#pragma unroll
      for (int rr = 0; rr < 2; rr++) { s1[rr] = wave_sum(s1[rr]); s2[rr] = wave_sum(s2[rr]); gs[rr] = wave_sum(gs[rr]); }
      const bool od = lane & 1;
      const float m1 = (od ? s1[1] : s1[0]) * (1.f / D), sq = (od ? s2[1] : s2[0]) * (1.f / D), gz = od ? gs[1] : gs[0], cc = od ? c0[1] : c0[0];
      float var = rbf(fmaxf_(__builtin_fmaf(-m1, m1, sq), 0.f));
      float den = rbf(__builtin_amdgcn_sqrtf(rbf(var + 1e-5f)));
      float g = rbf(__builtin_amdgcn_rcpf(1.f + __builtin_amdgcn_exp2f(rbf(gz) * -1.4426950408889634f)));
      if (lane < 2) *(f32x4*)(stt + 4 * (2 * a4 + lane)) = f32x4{rbf(cc + m1), den, __builtin_amdgcn_rcpf(den), g};
      if (w == 4 && lane == 0) ((unsigned*)stt)[32] = gen + 1;
    }
    group_barrier(ctl + CW_AB * 4, 4, lane);
    { unsigned t32 = __builtin_amdgcn_readfirstlane(gen) + 1; tag = ((u64)t32 << 32) | (unsigned)~t32; }
    {
      const int t = tid - 256;
      float h0[8], h1[8];
#pragma unroll
      for (int r = 0; r < 8; r++) {
        f32x4 sv = *(const f32x4*)(stt + 4 * r);
        float n0 = rbf(bflo(col[r]) - sv[0]), n1 = rbf(bflo(col1[r]) - sv[0]);
        float q0 = n0 * sv[2], q1 = n1 * sv[2];
        h0[r] = __builtin_fmaf(__builtin_fmaf(-q0, sv[1], n0), sv[2], q0);
        h1[r] = __builtin_fmaf(__builtin_fmaf(-q1, sv[1], n1), sv[2], q1);
      }
      u32x4 H0, H1, X0, X1;
#pragma unroll
      for (int i = 0; i < 4; i++) {
        H0[i] = pk_bf16(h0[2 * i], h0[2 * i + 1]); H1[i] = pk_bf16(h1[2 * i], h1[2 * i + 1]);
        X0[i] = col[2 * i] | (col[2 * i + 1] << 16);
        X1[i] = col1[2 * i] | (col1[2 * i + 1] << 16);
      }
      const int kk = s0 >> 4, hh = (s0 >> 3) & 1;
      __amdgpu_buffer_rsrc_t hr = mk_rsrc(ws + WS_HF, 4u << 20);
      unsigned ho = (((unsigned)(2 * a4) * 128 + kk) * 2 + (lane >> 5)) * 1024 + ((lane & 31) + 32 * hh) * 16;
      st16_sc1(hr, ho, H0); st16_sc1(hr, ho + 128 * 2048, H1);
      unsigned xo = (unsigned)WS_XC + ((unsigned)(s0 >> 3) * 512 + 128 * a4 + lane) * 16;
      st16_sc1(hr, xo, X0); st16_sc1(hr, xo + 1024, X1);
      if (t < 8) st4_sc1(mk_rsrc(ws + WS_G, 8192), (s0 + t) * 4, f2u(stt[4 * t + 3]));
    }
    wait_vm0();
    group_barrier(ctl + CW_AB * 4, 8, lane);
    if (w == 4) post_flag(ws, WS_F1, bid, tag, lane);
    __builtin_amdgcn_s_setprio(0);
    const float dtv = rbf(__builtin_amdgcn_exp2f(bflo(pre[6]) * 1.4426950408889634f));
    const unsigned pA = pre[0] | (pre[1] << 16), pB = pre[2] | (pre[3] << 16), pC = pre[4] | (pre[5] << 16);
    char* kcs = smem + LDS_AUX + kch * C_SIZE;
    char* kpar = kcs + C_PAR + (2 + (w & 1)) * 256;
    float* kpp = (float*)(kcs + C_KC) + (2 + (w & 1)) * 656;
    f32x4 kacc[3] = {};
#pragma unroll 1
    for (int u = 0; u < 4 - GU; u++)
      kgen_unit(kacc, kpar, lane, shfl_from(pA, lane, 16 * u), shfl_from(pB, lane, 16 * u), shfl_from(pC, lane, 16 * u), dtv);
    kgen_store(kacc, kpp, red + kch * 4 + 2 + (w & 1), lane);
    wait_lgkm0();
    if (lane == 0) lds_add1(ctl + CW_KG * 4);
    {
      const char* b2p = uni_ptr(P.w2 + (size_t)bid * 28672 + (w - 4) * 7168);
      const unsigned dst = sbase + LDS_AUX + (w & 2 ? C_SIZE : 0) + C_U16 + (w & 1) * 2048;
      dma16(dst, b2p, lane * 16); dma16(dst + 1024, b2p + 1024, lane * 16);
      dma16(dst, b2p + 2048, lane * 16); dma16(dst + 1024, b2p + 3072, lane * 16);
      dma16(dst, b2p + 4096, lane * 16); dma16(dst + 1024, b2p + 5120, lane * 16);
      dma16(dst, b2p + 6144, lane * 16);
    }
  }

  if (isg) {
    const int h = lane >> 5, blk = (lane >> 4) & 1, q = (lane & 15) >> 2, p = lane & 3;
    const unsigned rd0 = (8 * h + q) * 128 + 64 * (q >> 1) + 32 * blk + 8 * p;
    const unsigned ra0 = ring + rd0, ra1 = ring + (rd0 ^ 64);
    const int mi = w >> 1, ni = w & 1;
    unsigned gbt = 0;
    WFrag WR[NWR];
    const int f0 = ft * 64 + mi * 32 + 4 * h;
    u32x2 b1v[4];
#pragma unroll
    for (int G = 0; G < 4; G++) b1v[G] = *(const u32x2*)(P.b1 + f0 + 8 * G);
    unsigned pre[7];
    {
      const unsigned po = ((unsigned)kd * NM + 16 * GU * (w & 1) + (lane & 31)) * 2;
      glu16<0>(pre[0], P.Are, po); glu16<0>(pre[1], P.Aim, po); glu16<0>(pre[2], P.Bre, po); glu16<0>(pre[3], P.Bim, po);
      glu16<0>(pre[4], P.Cre, po); glu16<0>(pre[5], P.Cim, po); glu16<0>(pre[6], P.logdt, kd * 2);
    }
    wg_barrier();
    const unsigned wdst = sbase + LDS_AUX + (w >> 1) * C_SIZE + C_SK + (w & 1) * 2048;
    {
      constexpr size_t T0 = (size_t)(NWR + RING) * 64 * F * 2, TN = (size_t)L * F * 2;
      size_t o = T0 + (size_t)bid * 9216 + w * 2304;
      if (o + 3072 > TN) o = TN - 3072;
      const char* p1 = uni_ptr(P.w1 + (o & ~(size_t)15));
      static_for<0, 3>([&](auto ic) NOPK __attribute__((always_inline)) { constexpr int i = ic.value; dma16(wdst + (i & 1) * 1024, p1 + i * 1024, lane * 16); });
    }
    if (has1) static_for<0, RING>([&](auto ic) NOPK __attribute__((always_inline)) { constexpr int i = ic.value; issue_w(W1, i, ring + i * 2048); });
    else static_for<0, RING>([&](auto ic) NOPK __attribute__((always_inline)) { constexpr int i = ic.value; issue_w(W2, i, ring + i * 2048); });
    asm volatile("s_waitcnt vmcnt(%7)" : "+v"(pre[0]), "+v"(pre[1]), "+v"(pre[2]), "+v"(pre[3]), "+v"(pre[4]), "+v"(pre[5]), "+v"(pre[6]) : "i"(2 * RING + (WARM0 ? 3 : 0)) : "memory");
    {
      const float dtv = rbf(__builtin_amdgcn_exp2f(bflo(pre[6]) * 1.4426950408889634f));
      const unsigned pA = pre[0] | (pre[1] << 16), pB = pre[2] | (pre[3] << 16), pC = pre[4] | (pre[5] << 16);
      char* kcs = smem + LDS_AUX + kch * C_SIZE;
      char* kpar = kcs + C_PAR + (w & 1) * 256;
      f32x4 kacc[3] = {};
#pragma unroll 1
      for (int u = 0; u < GU; u++)
        kgen_unit(kacc, kpar, lane, shfl_from(pA, lane, 16 * u), shfl_from(pB, lane, 16 * u), shfl_from(pC, lane, 16 * u), dtv);
      kgen_store(kacc, (float*)(kcs + C_KC) + (w & 1) * 656, red + kch * 4 + (w & 1), lane);
      wait_lgkm0();
      if (lane == 0) lds_add1(ctl + CW_KG * 4);
    }
    wait_vm0();
    lds_wait_ge<4>(ctl + CW_AB * 4, 8);
    if (has1) preload_regs(WR, W1, ring, ra0, ra1); else preload_regs(WR, W2, ring, ra0, ra1);
    lds_wait_ge<4>(ctl + CW_AB * 4, 8);
    { unsigned t32 = __builtin_amdgcn_readfirstlane(((const unsigned*)stt)[32]); tag = ((u64)t32 << 32) | (unsigned)~t32; }
    if (w == 0) {
      poll_flags((const u64*)(ws + WS_F1) + xcd * 256, 256, tag, lane);
      if (lane == 0) lds_st(ctl + CW_F1 * 4, 1);
      if (bid == 0 && lane == 0) __hip_atomic_store((GAS unsigned*)(ws + WS_GEN), (unsigned)(tag >> 32), __ATOMIC_RELAXED, __HIP_MEMORY_SCOPE_AGENT);
    } else {
      lds_wait_ge<4>(ctl + CW_F1 * 4, 1);
      wait_vm0();
    }
    asm volatile("" : "+v"(b1v[0]), "+v"(b1v[1]), "+v"(b1v[2]), "+v"(b1v[3]));
    if (has1) {
      f32x16 acc[2][2] = {};
      const char* abase = uni_ptr(ws + WS_HF + ((size_t)dt1 * 128 + w) * 2048);
      __builtin_amdgcn_s_setprio(2);
      gemm_loop<32>(acc, WR, W1, abase, lane * 16, ring, ra0, ra1);
      static_assert(NWR == 10 && RING == 12, "phase-2 preload schedule");
      static_for<0, 6>([&](auto ic) NOPK __attribute__((always_inline)) { constexpr int i = ic.value; issue_w(W2, i, ring + i * 2048); });
      f32x16 r;
      gbt += 4;
      reduce_write(acc, smem, 6 * 2048, w, lane);
      reduce_read(acc, r, smem, 6 * 2048, w, lane, ctl + CW_GB * 4, gbt);
      unsigned pk[4][2];
#pragma unroll
      for (int G = 0; G < 4; G++) {
        float bv[4] = {bflo(b1v[G][0]), bfhi(b1v[G][0]), bflo(b1v[G][1]), bfhi(b1v[G][1])};
        float o[4];
#pragma unroll
        for (int j = 0; j < 4; j++) {
          float u = rbf(rbf(r[4 * G + j]) + bv[j]);
          float pq = __builtin_fmaf(u * u, 0.1029432397f, 2.302208198f);
          float t = __builtin_amdgcn_exp2f(u * pq);
          o[j] = __builtin_fmaf(-u, __builtin_amdgcn_rcpf(t + 1.f), u);
        }
        pk[G][0] = pk_bf16(o[0], o[1]); pk[G][1] = pk_bf16(o[2], o[3]);
      }
      __amdgpu_buffer_rsrc_t ur = mk_rsrc(ws + WS_UF, 2u << 20);
#pragma unroll
      for (int kp = 0; kp < 2; kp++) {
        auto s0_ = __builtin_amdgcn_permlane32_swap(pk[2 * kp][0], pk[2 * kp + 1][0], false, false);
        auto s1_ = __builtin_amdgcn_permlane32_swap(pk[2 * kp][1], pk[2 * kp + 1][1], false, false);
        u32x4 v = {s0_[0], s1_[0], s0_[1], s1_[1]};
        int kk = ft * 4 + mi * 2 + kp;
        st16_sc1(ur, (((unsigned)dt1 * 112 + kk) * 2 + ni) * 1024 + lane * 16, v);
      }
      asm volatile("s_waitcnt vmcnt(2)" ::: "memory");
      static_for<0, 6>([&](auto ic) NOPK __attribute__((always_inline)) {
        constexpr int j = ic.value;
        read_w<j>(WR[j], ra0, ra1);
        if constexpr (j % 3 == 2) { wait_lgkm_w<0>(WR[j]); wait_lgkm_w<0>(WR[j - 1]); wait_lgkm_w<0>(WR[j - 2]); }
      });
      static_for<6, 10>([&](auto ic) NOPK __attribute__((always_inline)) { constexpr int i = ic.value; issue_w(W2, i, ring + (i - 6) * 2048); });
      issue_w(W2, 16, ring + 4 * 2048); issue_w(W2, 17, ring + 5 * 2048);
      asm volatile("s_waitcnt vmcnt(12)" ::: "memory");
      gbt += 4;
      group_barrier(ctl + CW_GB * 4, gbt, lane);
      if (w == 0) post_flag(ws, WS_F2, dt1 * 32 + ft, tag, lane);
      __builtin_amdgcn_s_setprio(0);
      asm volatile("s_waitcnt vmcnt(0)" ::: "memory");
      static_for<6, 10>([&](auto ic) NOPK __attribute__((always_inline)) { constexpr int j = ic.value; read_w<j - 6>(WR[j], ra0, ra1); });
      static_for<18, 22>([&](auto ic) NOPK __attribute__((always_inline)) { constexpr int i = ic.value; issue_w(W2, i, ring + (i % 12) * 2048); });
      issue_w(W2, 10, ring + 10 * 2048); issue_w(W2, 11, ring + 11 * 2048);
      wait_lgkm_w<0>(WR[6]); wait_lgkm_w<0>(WR[7]); wait_lgkm_w<0>(WR[8]); wait_lgkm_w<0>(WR[9]);
      static_for<12, 16>([&](auto ic) NOPK __attribute__((always_inline)) { constexpr int i = ic.value; issue_w(W2, i, ring + (i % 12) * 2048); });
    }
    const int sq0 = st * 64 + mi * 32 + 4 * h;
    u32x2 b2v[4];
#pragma unroll
    for (int G = 0; G < 4; G++) b2v[G] = *(const u32x2*)(P.b2 + sq0 + 8 * G);
    if (w == 0) {
      poll_flags((const u64*)(ws + WS_F2) + xcd * 256 + dt2 * 32, 28, tag, lane);
      if (lane == 0) lds_st(ctl + CW_F2 * 4, 1);
    } else {
      lds_wait_ge<2>(ctl + CW_F2 * 4, 1);
      wait_vm0();
    }
    asm volatile("" : "+v"(b2v[0]), "+v"(b2v[1]), "+v"(b2v[2]), "+v"(b2v[3]));
    {
      f32x16 acc[2][2] = {};
      const char* abase = uni_ptr(ws + WS_UF + ((size_t)dt2 * 112 + w) * 2048);
      __builtin_amdgcn_s_setprio(2);
      gemm_loop<28>(acc, WR, W2, abase, lane * 16, ring, ra0, ra1);
      reduce_write(acc, smem, 0, w, lane);
      poll_flags((const u64*)(ws + WS_F3) + xcd * 256 + dt2 * 32, 32, tag, lane);
      const int d = dt2 * 64 + ni * 32 + (lane & 31);
      __amdgpu_buffer_rsrc_t ar = mk_rsrc(ws + WS_AT, 2u << 20);
      __amdgpu_buffer_rsrc_t gr = mk_rsrc(ws + WS_G, 8192);
      u32x2 av[4]; u32x4 gvv[4];
#pragma unroll
      for (int G = 0; G < 4; G++) {
        av[G] = ld8_sc1(ar, ((unsigned)d * L + sq0 + 8 * G) * 2);
        gvv[G] = ld16_sc1(gr, (sq0 + 8 * G) * 4);
      }
      f32x16 r;
      gbt += 4;
      reduce_read(acc, r, smem, 0, w, lane, ctl + CW_GB * 4, gbt);
      u16* tl = (u16*)(smem + w * (RING * 2048) + 12288);
#pragma unroll
      for (int G = 0; G < 4; G++) {
        float bv[4] = {bflo(b2v[G][0]), bfhi(b2v[G][0]), bflo(b2v[G][1]), bfhi(b2v[G][1])};
        float aa[4] = {bflo(av[G][0]), bfhi(av[G][0]), bflo(av[G][1]), bfhi(av[G][1])};
#pragma unroll
        for (int j = 0; j < 4; j++) {
          float g = u2f(gvv[G][j]);
          float b = rbf(rbf(r[4 * G + j]) + bv[j]);
          float o = rbf(g * aa[j]) + rbf(rbf(1.f - g) * b);
          tl[(8 * G + 4 * h + j) * 40 + (lane & 31)] = (u16)(pk_bf16(o, 0.f) & 0xffff);
        }
      }
      wait_lgkm0();
      __builtin_amdgcn_wave_barrier();
#pragma unroll
      for (int it = 0; it < 2; it++) {
        int rr = it * 16 + (lane >> 2), cc = (lane & 3) * 8;
        u32x4 v = *(const u32x4*)(tl + rr * 40 + cc);
        *(u32x4*)(P.out + (size_t)(st * 64 + mi * 32 + rr) * D + dt2 * 64 + ni * 32 + cc) = v;
      }
    }
  } else {
    const int aw = w - 4;
    unsigned abt = 8;
    const int chl = aw >> 1, hw = aw & 1;
    const int d = cpair * 2 + chl;
    char* cs = smem + LDS_AUX + chl * C_SIZE;
    const unsigned pb = ctl + (CW_P0 + chl) * 4;
    unsigned pbt = 0;
    const int r = lane & 15, g = lane >> 4;
    const float dsk = bflo(P.Dskip[d]);
    lds_wait_ge(ctl + CW_KG * 4, 8);
    int sk_;
    {
      float bound = (u2f(red[chl * 4]) + u2f(red[chl * 4 + 1])) + (u2f(red[chl * 4 + 2]) + u2f(red[chl * 4 + 3]));
      unsigned kb = f2u(bound);
      sk_ = kb ? 14 - ((int)(kb >> 23) - 127) : 0;
      const float* kp = (const float*)(cs + C_KC);
      u16* kh = (u16*)(cs + C_KH);
#pragma unroll
      for (int i = 0; i < 6; i++) {
        int idx = (hw + 2 * i) * 64 + lane;
        if (idx < 641) {
          _Float16 hv = (_Float16)__builtin_amdgcn_ldexpf((kp[idx] + kp[656 + idx]) + (kp[1312 + idx] + kp[1968 + idx]), sk_);
          kh[idx] = __builtin_bit_cast(u16, hv);
        }
      }
    }
    pbt += 2;
    group_barrier(pb, pbt, lane);
    {
      const u16* kh = (const u16*)(cs + C_KH);
      if (lane < 8 && hw == 0) {
        *(u32x4*)(cs + C_KC + (lane >> 1) * 4160 + (lane & 1) * 16) = u32x4{0, 0, 0, 0};
      }
#pragma unroll
      for (int it = 0; it < 2; it++) {
        int c = hw * 128 + it * 64 + lane;
        int X = 8 * c;
        unsigned kvv[11];
#pragma unroll
        for (int j = 0; j < 11; j++) {
          int l = X - 3 + j;
          kvv[j] = kh[compact_idx(max(l, 0))];
        }
        if (c == 0) { kvv[0] = 0; kvv[1] = 0; kvv[2] = 0; }
#pragma unroll
        for (int q = 0; q < 4; q++) {
          u32x4 o;
#pragma unroll
          for (int e = 0; e < 4; e++) o[e] = kvv[3 - q + 2 * e] | (kvv[3 - q + 2 * e + 1] << 16);
          *(u32x4*)(cs + C_KC + q * 4160 + 32 + c * 16) = o;
        }
      }
    }
    lds_wait_ge<2>(ctl + CW_F1 * 4, 1);
    wait_vm0();
    int su;
    {
      __amdgpu_buffer_rsrc_t xr_ = mk_rsrc(ws + WS_XC, 2u << 20);
      u32x4 xr[2];
#pragma unroll
      for (int i = 0; i < 2; i++) xr[i] = ld16_x(xr_, ((unsigned)(hw * 128 + i * 64 + lane) * 512 + d) * 16);
      unsigned umax = 0;
#pragma unroll
      for (int i = 0; i < 2; i++)
#pragma unroll
        for (int j = 0; j < 4; j++) { umax = max(umax, (xr[i][j] & 0x7fff)); umax = max(umax, (xr[i][j] >> 16) & 0x7fff); }
      umax = wave_umax(umax);
      unsigned* red = (unsigned*)(cs + C_PAR + 1024);
      if (lane == 0) red[hw] = umax;
      pbt += 2;
      group_barrier(pb, pbt, lane);
      umax = max(red[0], red[1]);
      int eu = (int)(umax >> 7) - 127;
      su = umax ? 14 - eu : 0;
      u16* u16p = (u16*)(cs + C_U16);
      u16* skp = (u16*)(cs + C_SK);
      if (hw == 0 && lane < 32) *(u32x4*)(u16p + lane * 8) = u32x4{0, 0, 0, 0};
#pragma unroll
      for (int i = 0; i < 2; i++) {
        int sb = hw * 128 + i * 64 + lane;
        u32x4 o, sk;
#pragma unroll
        for (int j = 0; j < 4; j++) {
          float a = bflo(xr[i][j]), b = bfhi(xr[i][j]);
          o[j] = __builtin_bit_cast(unsigned, __builtin_amdgcn_cvt_pkrtz(__builtin_amdgcn_ldexpf(a, su), __builtin_amdgcn_ldexpf(b, su)));
          sk[j] = pk_bf16(a * dsk, b * dsk);
        }
        *(u32x4*)(u16p + 256 + sb * 8) = o;
        *(u32x4*)(skp + sb * 8) = sk;
      }
    }
    pbt += 2;
    group_barrier(pb, pbt, lane);
    f32x4 yacc[8] = {};
    {
      const int qq = (lane & 15) >> 2, pp = lane & 3;
      const unsigned cb = (unsigned)(size_t)(LAS char*)cs;
      const unsigned aaddr = cb + C_KC + qq * 4160 + (16 - 8 * g + 4 * pp + 16) * 2;
      const unsigned baddr = cb + C_U16 + (256 - 16 + 16 * r + 8 * g) * 2;
#pragma unroll
      for (int rr = 0; rr < 4; rr++) {
        const int rho = hw * 4 + rr;
        f16x8 af[8];
#pragma unroll
        for (int al = 0; al < 8; al++) {
          unsigned ad = aaddr + (rho + 8 * al) * 64;
          s16x4 a0 = tr_read(ad), a1 = tr_read(ad - 8);
          af[al] = __builtin_bit_cast(f16x8, __builtin_shufflevector(a0, a1, 0, 1, 2, 3, 4, 5, 6, 7));
        }
#pragma unroll
        for (int mu = 0; mu < 8; mu++) {
          f16x8 bf = *(const LAS f16x8*)(size_t)(baddr + (8 * mu - rho) * 64);
#pragma unroll
          for (int al = 0; al + mu < 8; al++)
            yacc[mu + al] = __builtin_amdgcn_mfma_f32_16x16x32_f16(af[al], bf, yacc[mu + al], 0, 0, 0);
        }
      }
    }
    {
      pbt += 2;
      group_barrier(pb, pbt, lane);
      f32x4* ex = (f32x4*)(cs + C_KC);
      f32x4 mine[4];
#pragma unroll
      for (int i = 0; i < 4; i++) {
        f32x4 snd;
#pragma unroll
        for (int e = 0; e < 4; e++) {
          float a = yacc[i][e], b = yacc[4 + i][e];
          asm volatile("" : "+v"(a), "+v"(b));
          snd[e] = hw ? a : b; mine[i][e] = hw ? b : a;
        }
        ex[(hw * 4 + i) * 64 + lane] = snd;
      }
      pbt += 2;
      group_barrier(pb, pbt, lane);
      __amdgpu_buffer_rsrc_t ar_ = mk_rsrc(ws + WS_AT, 2u << 20);
      const int sc = -(su + sk_);
#pragma unroll
      for (int i = 0; i < 4; i++) {
        int ti = hw * 4 + i;
        f32x4 o = ex[((1 - hw) * 4 + i) * 64 + lane];
        f32x4 me = mine[i];
        int tt = 256 * ti + 16 * r + 4 * g;
        float y0 = rbf(__builtin_amdgcn_ldexpf(me[0] + o[0], sc)), y1 = rbf(__builtin_amdgcn_ldexpf(me[1] + o[1], sc));
        float y2 = rbf(__builtin_amdgcn_ldexpf(me[2] + o[2], sc)), y3 = rbf(__builtin_amdgcn_ldexpf(me[3] + o[3], sc));
        u32x2 sk = *(const u32x2*)((const u16*)(cs + C_SK) + tt);
        u32x2 res = {pk_bf16(y0 + bflo(sk[0]), y1 + bfhi(sk[0])), pk_bf16(y2 + bflo(sk[1]), y3 + bfhi(sk[1]))};
        st8_sc1(ar_, ((unsigned)d * L + tt) * 2, res);
      }
      wait_vm0();
      abt += 4;
      group_barrier(ctl + CW_AB * 4, abt, lane);
      if (aw == 0) post_flag(ws, WS_F3, cpair, tag, lane);
    }
  }
}

extern "C" void kernel_launch(void* const* d_in, const int* in_sizes, int n_in,
                              void* d_out, int out_size, void* d_ws, size_t ws_size,
                              hipStream_t stream) {
  (void)hipFuncSetAttribute((const void*)mega_kernel, hipFuncAttributeMaxDynamicSharedMemorySize, LDS_TOTAL);
  Params P;
  P.x = (const u16*)d_in[0]; P.Are = (const u16*)d_in[1]; P.Aim = (const u16*)d_in[2]; P.Bre = (const u16*)d_in[3]; P.Bim = (const u16*)d_in[4];
  P.Cre = (const u16*)d_in[5]; P.Cim = (const u16*)d_in[6]; P.logdt = (const u16*)d_in[7]; P.Dskip = (const u16*)d_in[8];
  P.b1 = (const u16*)d_in[11]; P.b2 = (const u16*)d_in[13]; P.gw = (const u16*)d_in[14];
  P.w1 = (const char*)d_in[10]; P.w2 = (const char*)d_in[12];
  P.ws = (char*)d_ws; P.out = (u16*)d_out;
  mega_kernel<<<256, 512, LDS_TOTAL, stream>>>(P);
}
```
